```python
import torch

def _softmax(x, axis=-1):
    return torch.softmax(x, dim=axis)
def _gelu(x):
    return torch.nn.functional.gelu(x, approximate="tanh")
def _silu(x):
    return torch.nn.functional.silu(x)
def _sigmoid(x):
    return torch.sigmoid(x)
def _layernorm(x, axis=-1, eps=1e-5):
    m = x.mean(dim=axis, keepdim=True)
    v = x.var(dim=axis, keepdim=True, unbiased=False)
    return (x - m) / torch.sqrt(v + eps)
def _scatter_add(tgt, idx, src, axis=0):
    return tgt.clone().index_add_(axis, idx, src)
def _segment_sum(src, seg, n):
    out = torch.zeros((n,) + src.shape[1:], dtype=src.dtype)
    return out.index_add_(0, seg, src)
def _pad(x, pw):
    flat = tuple(v for pair in reversed(pw) for v in pair)
    return torch.nn.functional.pad(x, flat)

kernel_name = 'recurrent_scan__gqa_block_cached__mixer_channel_mlp__decode'

def setup_inputs():

    _w = (lambda a: (lambda t: t.to(torch.bfloat16) if t.is_floating_point() else t)(torch.as_tensor(a)))
    def _A_setup():
        B, Sq, D = 4, 1, 512
        Hq, Hkv, Sk, Dh = 32, 8, 16384, 128
        assert Hq % Hkv == 0, (Hq, Hkv)
        return {
            "x": _w((lambda *s: torch.randn(*s, dtype=torch.bfloat16) * 0.1)(B, Sq, D)),
            "k_cache": _w((lambda *s: torch.randn(*s, dtype=torch.bfloat16) * 0.8)(B, Hkv, Sk, Dh)),
            "v_cache": _w((lambda *s: torch.randn(*s, dtype=torch.bfloat16) * 3.2)(B, Hkv, Sk, Dh)),
            "wq": _w((lambda *s: torch.randn(*s, dtype=torch.bfloat16) * 0.8)(D, Hq * Dh) * (D ** -0.5)),
            "wo": _w((lambda *s: torch.randn(*s, dtype=torch.bfloat16) * 3.2)(Hq * Dh, D) * ((Hq * Dh) ** -0.5)),
        }
    _A_in = _A_setup()

    def _B_setup():
        B, S, D, F = 4, 1, 512, 1792
        return {
            "x": _w((lambda *s: torch.randn(*s, dtype=torch.bfloat16) * 0.1)(B, S, D)),
            "w1": _w((lambda *s: torch.randn(*s, dtype=torch.bfloat16) * 3.2)(D, F) * (D ** -0.5)),
            "b1": _w((lambda *s: torch.randn(*s, dtype=torch.bfloat16) * 3.2)(F)),
            "w2": _w((lambda *s: torch.randn(*s, dtype=torch.bfloat16) * 3.2)(F, D) * (F ** -0.5)),
            "b2": _w((lambda *s: torch.randn(*s, dtype=torch.bfloat16) * 3.2)(D)),
        }
    _B_in = _B_setup()

    return {
        "A_x": _A_in["x"],
        "A_k_cache": _A_in["k_cache"],
        "A_v_cache": _A_in["v_cache"],
        "A_wq": _A_in["wq"],
        "A_wo": _A_in["wo"],
        "B_x": _B_in["x"],
        "B_w1": _B_in["w1"],
        "B_b1": _B_in["b1"],
        "B_w2": _B_in["w2"],
        "B_b2": _B_in["b2"],
    }

def reference(A_x, A_k_cache, A_v_cache, A_wq, A_wo, B_x, B_w1, B_b1, B_w2, B_b2):
    def _A_ref(x, k_cache, v_cache, wq, wo):
        B, Sq, D = x.shape
        Hq, Dh = 32, 128
        Hkv = k_cache.shape[1]
        rep = Hq // Hkv
        q = (lambda x, *d: x.permute(*d))((x @ wq).reshape(B, Sq, Hq, Dh), 0, 2, 1, 3)
        k = (lambda x, n, axis: torch.repeat_interleave(x, n, dim=axis))(k_cache, rep, axis=1)
        v = (lambda x, n, axis: torch.repeat_interleave(x, n, dim=axis))(v_cache, rep, axis=1)
        scores = torch.einsum("bhqd,bhkd->bhqk", q, k) * (Dh ** -0.5)
        p = _softmax(scores, axis=-1)
        o = torch.einsum("bhqk,bhkd->bhqd", p, v)
        return (lambda x, *d: x.permute(*d))(o, 0, 2, 1, 3).reshape(B, Sq, Hq * Dh) @ wo

    def _B_ref(x, w1, b1, w2, b2):
        y = _layernorm(x, axis=-1)
        h = _gelu(torch.einsum("bsd,df->bsf", y, w1) + b1)
        return torch.einsum("bsf,fd->bsd", h, w2) + b2

    x = A_x
    xn = _layernorm(x, axis=-1)
    s = _A_ref(xn, A_k_cache, A_v_cache, A_wq, A_wo)
    g = _silu(_B_ref(xn, B_w1, B_b1, B_w2, B_b2))
    return x + s * g
```

```cpp
#include <hip/hip_runtime.h>
#include <hip/hip_bf16.h>
#include <stdint.h>

#define DYN 1

typedef unsigned u32x4 __attribute__((ext_vector_type(4)));
typedef unsigned u32x2 __attribute__((ext_vector_type(2)));
typedef float f32x4 __attribute__((ext_vector_type(4)));
typedef __bf16 bf16x2 __attribute__((ext_vector_type(2)));
typedef unsigned short u16;
typedef unsigned long long u64;
#define GAS __attribute__((address_space(1)))
#define RLX_AGENT __ATOMIC_RELAXED, __HIP_MEMORY_SCOPE_AGENT

constexpr int NB = 4, DM = 512, HQ = 32, HKV = 8, SK = 16384, DH = 128, FF = 1792;
constexpr int NWG = 256, NW = 16, NT = NW * 64;
constexpr int PAGES = SK / 16;
constexpr int AUX_NT = 2, AUX_SC1 = 16;
constexpr int RSRC_W3 = 0x00020000;
constexpr int WS_EPOCH = 0;
constexpr int WS_CNT = 4096;
constexpr int WS_QG = 16384;
constexpr int WS_HG = WS_QG + NB * 4096 * 8;
constexpr int WS_ML = WS_HG + NB * FF * 8;
constexpr int WS_PG = WS_ML + NB * HQ * 8 * 2 * 8;
constexpr int WS_SG = WS_PG + NB * HQ * 8 * 128 * 8;
constexpr int WS_MG = WS_SG + 64 * NB * DM * 8;
constexpr int WS_END = WS_MG + 32 * NB * DM * 8;
constexpr int WS_STAMP = 8 << 20;
constexpr int OOB = 0x7ff00000;
constexpr int L_WOT = 0;
constexpr int L_W2T = L_WOT + 64 * 272;
constexpr int L_MRG = L_W2T + 64 * 144;
constexpr int L_MM = L_MRG + NW * 512 * 4;
constexpr int L_LL = L_MM + 256;
constexpr int L_RQ = L_LL + 256;
constexpr int L_RH = L_RQ + 8 * 64 * 4;
constexpr int L_Q = L_RH + 8 * 32 * 4;
constexpr int L_O = L_Q + 1024;
constexpr int L_END = L_O + 12 * 128;
constexpr unsigned SPIN_CAP = 1u << 16;

struct Params {
  const u16* x; const u16* kc; const u16* vc; const u16* wq; const u16* wo;
  const u16* w1; const u16* b1; const u16* w2; const u16* b2;
  u16* out; unsigned char* ws;
};

__device__ __forceinline__ float dot2(unsigned a, unsigned b, float acc) {
  return __builtin_amdgcn_fdot2_f32_bf16(__builtin_bit_cast(bf16x2, a), __builtin_bit_cast(bf16x2, b), acc, false);
}
template <int CTRL> __device__ __forceinline__ float dpp(float x) {
  return __builtin_bit_cast(float, __builtin_amdgcn_mov_dpp(__builtin_bit_cast(int, x), CTRL, 0xf, 0xf, true));
}
constexpr int XOR1 = 0xB1, XOR2 = 0x4E, XOR7 = 0x141, XOR8 = 0x128, ROR4 = 0x124, NEWBC = 0x150;
__device__ __forceinline__ float bfr(float x) { return (float)(__bf16)x; }
__device__ __forceinline__ unsigned bfbits(float x) { return (unsigned)__builtin_bit_cast(u16, (__bf16)x); }
__device__ __forceinline__ float bflo(unsigned u) { return __builtin_bit_cast(float, u << 16); }
__device__ __forceinline__ float bfhi(unsigned u) { return __builtin_bit_cast(float, u & 0xffff0000u); }
__device__ __forceinline__ float swadd32(float a, float b) {
  auto s = __builtin_amdgcn_permlane32_swap(__float_as_uint(a), __float_as_uint(b), false, false);
  return __uint_as_float(s[0]) + __uint_as_float(s[1]);
}
__device__ __forceinline__ float swadd16(float a, float b) {
  auto s = __builtin_amdgcn_permlane16_swap(__float_as_uint(a), __float_as_uint(b), false, false);
  return __uint_as_float(s[0]) + __uint_as_float(s[1]);
}
__device__ __forceinline__ float swmax32(float a) {
  auto s = __builtin_amdgcn_permlane32_swap(__float_as_uint(a), __float_as_uint(a), false, false);
  return fmaxf(__uint_as_float(s[0]), __uint_as_float(s[1]));
}
__device__ __forceinline__ float swmax16(float a) {
  auto s = __builtin_amdgcn_permlane16_swap(__float_as_uint(a), __float_as_uint(a), false, false);
  return fmaxf(__uint_as_float(s[0]), __uint_as_float(s[1]));
}
__device__ __forceinline__ float wave_sum(float v) {
  v += dpp<XOR1>(v); v += dpp<XOR2>(v); v += dpp<XOR7>(v); v += dpp<XOR8>(v);
  v = swadd16(v, v); v = swadd32(v, v);
  return v;
}
template <int CTRL> __device__ __forceinline__ float trsel(float lo, float hi, bool sel) {
  const float t1 = lo + dpp<CTRL>(lo), t2 = hi + dpp<CTRL>(hi);
  return sel ? t2 : t1;
}
__device__ __forceinline__ float divr(float a, float d, float r) {
  const float q = a * r;
  return fmaf(fmaf(-q, d, a), r, q);
}
__device__ __forceinline__ u32x2 gpack(float v, unsigned tlo, unsigned thi) {
  const unsigned b = __float_as_uint(v) + 0x80u;
  return u32x2{(b >> 8) | tlo, thi};
}
__device__ __forceinline__ unsigned gbad(unsigned lo, unsigned hi, unsigned tlo, unsigned thi) {
  return (hi ^ thi) | ((lo ^ tlo) & 0xff000000u);
}
__device__ __forceinline__ float gval(unsigned lo) { return __uint_as_float(lo << 8); }
#define LDS_BARRIER() do { asm volatile("s_waitcnt lgkmcnt(0)" ::: "memory"); __builtin_amdgcn_s_barrier(); asm volatile("" ::: "memory"); } while (0)

#define STAMP(i) do {} while (0)

__device__ __forceinline__ float row_sum(float v) {
  v += dpp<XOR1>(v); v += dpp<XOR2>(v); v += dpp<XOR7>(v); v += dpp<XOR8>(v);
  return v;
}
__device__ __forceinline__ void ln_stats(const float (&v)[32], float& mb, float& den, float& rc) {
  float s0 = 0.f, s1 = 0.f, s2 = 0.f, s3 = 0.f;
#pragma unroll
  for (int i = 0; i < 32; i += 4) { s0 += v[i]; s1 += v[i + 1]; s2 += v[i + 2]; s3 += v[i + 3]; }
  const float mean = row_sum((s0 + s1) + (s2 + s3)) * (1.0f / DM);
  float q0 = 0.f, q1 = 0.f;
#pragma unroll
  for (int i = 0; i < 32; i += 2) { const float d0 = v[i] - mean, d1 = v[i + 1] - mean; q0 = fmaf(d0, d0, q0); q1 = fmaf(d1, d1, q1); }
  const float var = row_sum(q0 + q1) * (1.0f / DM);
  mb = bfr(mean);
  den = bfr(__builtin_amdgcn_sqrtf(bfr(bfr(var) + 1e-5f)));
  rc = __builtin_amdgcn_rcpf(den);
}
__device__ __forceinline__ float rdl(float v, int l) { return __builtin_bit_cast(float, __builtin_amdgcn_readlane(__builtin_bit_cast(int, v), l)); }

__global__ void __launch_bounds__(NT, 4) fused_decode_kernel(Params p) {
  __shared__ __attribute__((aligned(16))) unsigned char lds[L_END];
  const int bid = blockIdx.x, tid = threadIdx.x, lane = tid & 63;
  const int w = __builtin_amdgcn_readfirstlane(tid >> 6);
  const int r = lane >> 4, c = lane & 15;
  const int xg = bid & 7, yg = bid >> 3;
  STAMP(0);
  const __amdgpu_buffer_rsrc_t rws = __builtin_amdgcn_make_buffer_rsrc((void*)p.ws, 0, WS_END, RSRC_W3);
  const __amdgpu_buffer_rsrc_t rx = __builtin_amdgcn_make_buffer_rsrc((void*)p.x, 0, NB * DM * 2, RSRC_W3);
  const int ab = yg >> 3, akv = yg & 7;
  const size_t kvbase = (size_t)(ab * HKV + akv) * SK * DH;
  const __amdgpu_buffer_rsrc_t rk = __builtin_amdgcn_make_buffer_rsrc((void*)(p.kc + kvbase), 0, PAGES * 4096, RSRC_W3);
  const __amdgpu_buffer_rsrc_t rv = __builtin_amdgcn_make_buffer_rsrc((void*)(p.vc + kvbase), 0, PAGES * 4096, RSRC_W3);
  const int voff = lane * 16;

  const int qj = xg * 32 + yg;
  if (w == 0 && (qj & 7) == 0 && lane == 0)
    __hip_atomic_store((GAS unsigned*)(p.ws + WS_CNT + ((((qj & 31) >> 3) << 3) + (qj >> 5)) * 256), (unsigned)(2 * 8 * NW), RLX_AGENT);
  const u64 epoch = __hip_atomic_load((GAS u64*)(p.ws + WS_EPOCH), RLX_AGENT);
  u32x4 xc[4];
#pragma unroll
  for (int i = 0; i < 4; ++i) xc[i] = __builtin_amdgcn_raw_buffer_load_b128(rx, (r * DM + c * 8 + i * 128) * 2, 0, 0);
  const int half = lane & 1, rp = lane >> 1, w8 = w & 7;
  const int r0 = w8 * 64 + rp, r1 = r0 + 32;
  unsigned xs0[4], xs1[4];
#pragma unroll
  for (int b = 0; b < 4; ++b) {
    xs0[b] = __builtin_amdgcn_raw_buffer_load_b16(rx, (b * DM + r0) * 2, 0, 0);
    xs1[b] = __builtin_amdgcn_raw_buffer_load_b16(rx, (b * DM + r1) * 2, 0, 0);
  }
  const bool has_mlp = yg < 28;
  const int cm = xg * 28 + yg;
  unsigned b1v;
  {
    const __amdgpu_buffer_rsrc_t rb = __builtin_amdgcn_make_buffer_rsrc((void*)p.b1, 0, FF * 2, RSRC_W3);
    b1v = __builtin_amdgcn_raw_buffer_load_b16(rb, (has_mlp && w == 8) ? (cm * 8 + (lane & 7)) * 2 : OOB, 0, 0);
  }
  u32x4 wa, wb;
  if (w < 8) {
    const __amdgpu_buffer_rsrc_t rq = __builtin_amdgcn_make_buffer_rsrc((void*)p.wq, 0, DM * 4096 * 2, RSRC_W3);
    wa = __builtin_amdgcn_raw_buffer_load_b128(rq, (r0 * 4096 + qj * 16 + half * 8) * 2, 0, 0);
    wb = __builtin_amdgcn_raw_buffer_load_b128(rq, (r1 * 4096 + qj * 16 + half * 8) * 2, 0, 0);
  } else {
    const __amdgpu_buffer_rsrc_t r1s = __builtin_amdgcn_make_buffer_rsrc((void*)p.w1, 0, DM * FF * 2, RSRC_W3);
    const int o = has_mlp ? (cm * 8 + half * 4) * 2 : OOB;
    const u32x2 a = __builtin_amdgcn_raw_buffer_load_b64(r1s, r0 * FF * 2 + o, 0, 0);
    const u32x2 b = __builtin_amdgcn_raw_buffer_load_b64(r1s, r1 * FF * 2 + o, 0, 0);
    wa = u32x4{a[0], a[1], 0u, 0u}; wb = u32x4{b[0], b[1], 0u, 0u};
  }
  const int oh = yg, onc = xg;
  u32x4 wov, w2v;
  {
    const __amdgpu_buffer_rsrc_t ro = __builtin_amdgcn_make_buffer_rsrc((void*)p.wo, 0, 4096 * DM * 2, RSRC_W3);
    wov = __builtin_amdgcn_raw_buffer_load_b128(ro, ((oh * 128 + (tid >> 3)) * DM + onc * 64 + (tid & 7) * 8) * 2, 0, AUX_NT);
    const __amdgpu_buffer_rsrc_t r2 = __builtin_amdgcn_make_buffer_rsrc((void*)p.w2, 0, FF * DM * 2, RSRC_W3);
    w2v = __builtin_amdgcn_raw_buffer_load_b128(r2, tid < 448 ? ((oh * 56 + (tid >> 3)) * DM + onc * 64 + (tid & 7) * 8) * 2 : OOB, 0, AUX_NT);
  }
  __builtin_amdgcn_sched_barrier(0);
  __builtin_amdgcn_s_barrier();
  __builtin_amdgcn_sched_barrier(0);
  u32x4 kr[4], vr[4];
  {
    const int so = w >= 8 ? (xg * NW + w) * 4096 : OOB;
#pragma unroll
    for (int t = 0; t < 4; ++t) kr[t] = __builtin_amdgcn_raw_buffer_load_b128(rk, voff + t * 1024, so, AUX_NT);
#pragma unroll
    for (int t = 0; t < 4; ++t) vr[t] = __builtin_amdgcn_raw_buffer_load_b128(rv, voff + t * 1024, so, AUX_NT);
  }
  __builtin_amdgcn_sched_barrier(0);
  STAMP(1);

  const u64 tg = ((epoch + 1ull) * 0x9E3779B97F4A7C15ull) >> 24;
  const unsigned tlo = __builtin_amdgcn_readfirstlane((unsigned)(tg & 0xffu) << 24);
  const unsigned thi = __builtin_amdgcn_readfirstlane((unsigned)(tg >> 8));
  unsigned apk[4];
  {
    float v[32];
#pragma unroll
    for (int i = 0; i < 16; ++i) { v[2 * i] = bflo(xc[i >> 2][i & 3]); v[2 * i + 1] = bfhi(xc[i >> 2][i & 3]); }
    float mb, den, rc;
    ln_stats(v, mb, den, rc);
    float a0[4], a1[4];
#pragma unroll
    for (int b = 0; b < 4; ++b) {
      const float mbb = rdl(mb, 16 * b), denb = rdl(den, 16 * b), rcb = rdl(rc, 16 * b);
      a0[b] = bfr(divr(bfr(bflo(xs0[b]) - mbb), denb, rcb));
      a1[b] = bfr(divr(bfr(bflo(xs1[b]) - mbb), denb, rcb));
    }
    if (w >= 8) {
#pragma unroll
      for (int i = 0; i < 32; ++i) v[i] = bfr(divr(bfr(v[i] - mb), den, rc));
      ln_stats(v, mb, den, rc);
#pragma unroll
      for (int b = 0; b < 4; ++b) {
        const float mbb = rdl(mb, 16 * b), denb = rdl(den, 16 * b), rcb = rdl(rc, 16 * b);
        a0[b] = bfr(divr(bfr(a0[b] - mbb), denb, rcb));
        a1[b] = bfr(divr(bfr(a1[b] - mbb), denb, rcb));
      }
    }
#pragma unroll
    for (int b = 0; b < 4; ++b) apk[b] = (__float_as_uint(a0[b]) >> 16) | (__float_as_uint(a1[b]) & 0xffff0000u);
  }
  STAMP(2);
  const bool b1s = lane & 2, b2s = lane & 4, b3s = lane & 8;
  if (w < 8) {
    float y[32];
#pragma unroll
    for (int k = 0; k < 4; ++k) {
      const unsigned lo = __builtin_amdgcn_perm(wb[k], wa[k], 0x05040100u), hi = __builtin_amdgcn_perm(wb[k], wa[k], 0x07060302u);
#pragma unroll
      for (int b = 0; b < 4; ++b) { y[b * 8 + 2 * k] = dot2(lo, apk[b], 0.f); y[b * 8 + 2 * k + 1] = dot2(hi, apk[b], 0.f); }
    }
#pragma unroll
    for (int h = 0; h < 16; ++h) y[h] = swadd32(y[h], y[h + 16]);
#pragma unroll
    for (int h = 0; h < 8; ++h) y[h] = swadd16(y[h], y[h + 8]);
#pragma unroll
    for (int h = 0; h < 4; ++h) y[h] = trsel<ROR4>(y[h], y[h + 4], b2s);
#pragma unroll
    for (int h = 0; h < 2; ++h) y[h] = trsel<XOR8>(y[h], y[h + 2], b3s);
    y[0] = trsel<XOR2>(y[0], y[1], b1s);
    const int col = half * 8 + (b2s ? 4 : 0) + (b3s ? 2 : 0) + (b1s ? 1 : 0);
    ((float*)(lds + L_RQ))[w8 * 64 + r * 16 + col] = y[0];
  } else {
    float y[16];
#pragma unroll
    for (int k = 0; k < 2; ++k) {
      const unsigned lo = __builtin_amdgcn_perm(wb[k], wa[k], 0x05040100u), hi = __builtin_amdgcn_perm(wb[k], wa[k], 0x07060302u);
#pragma unroll
      for (int b = 0; b < 4; ++b) { y[b * 4 + 2 * k] = dot2(lo, apk[b], 0.f); y[b * 4 + 2 * k + 1] = dot2(hi, apk[b], 0.f); }
    }
#pragma unroll
    for (int h = 0; h < 8; ++h) y[h] = swadd32(y[h], y[h + 8]);
#pragma unroll
    for (int h = 0; h < 4; ++h) y[h] = swadd16(y[h], y[h + 4]);
#pragma unroll
    for (int h = 0; h < 2; ++h) y[h] = trsel<ROR4>(y[h], y[h + 2], b2s);
    y[0] = trsel<XOR8>(y[0], y[1], b3s);
    y[0] += dpp<XOR2>(y[0]);
    const int col = half * 4 + (b2s ? 2 : 0) + (b3s ? 1 : 0);
    if (!b1s) ((float*)(lds + L_RH))[w8 * 32 + r * 8 + col] = y[0];
  }
  LDS_BARRIER();
  STAMP(3);
  if (w == 0) {
    float s = 0.f;
#pragma unroll
    for (int i = 0; i < 8; ++i) s += ((const float*)(lds + L_RQ))[i * 64 + lane];
    __builtin_amdgcn_raw_buffer_store_b64(gpack(bfr(s), tlo, thi), rws, WS_QG + (r * 4096 + qj * 16 + c) * 8, 0, AUX_SC1);
  }
  {
    u16* wt = (u16*)(lds + L_WOT) + ((tid & 7) * 8) * 136 + (tid >> 3);
#pragma unroll
    for (int k = 0; k < 4; ++k) { wt[(2 * k) * 136] = (u16)(wov[k] & 0xffffu); wt[(2 * k + 1) * 136] = (u16)(wov[k] >> 16); }
    if (tid < 448) {
      u16* w2t = (u16*)(lds + L_W2T) + ((tid & 7) * 8) * 72 + (tid >> 3);
#pragma unroll
      for (int k = 0; k < 4; ++k) { w2t[(2 * k) * 72] = (u16)(w2v[k] & 0xffffu); w2t[(2 * k + 1) * 72] = (u16)(w2v[k] >> 16); }
    }
  }
  STAMP(4);
  u32x4 qg[4];
  const int qgo = WS_QG + (ab * 4096 + akv * 512) * 8 + lane * 16;
  if (w == 0) {
#pragma unroll
    for (int i = 0; i < 4; ++i) qg[i] = __builtin_amdgcn_raw_buffer_load_b128(rws, qgo + i * 1024, 0, AUX_SC1);
  }
  __builtin_amdgcn_sched_barrier(0);
  __builtin_amdgcn_s_barrier();
  __builtin_amdgcn_sched_barrier(0);
  if (w >= 1 && w < 8) {
    const int so = (xg * NW + w) * 4096;
#pragma unroll
    for (int t = 0; t < 4; ++t) kr[t] = __builtin_amdgcn_raw_buffer_load_b128(rk, voff + t * 1024, so, AUX_NT);
#pragma unroll
    for (int t = 0; t < 4; ++t) vr[t] = __builtin_amdgcn_raw_buffer_load_b128(rv, voff + t * 1024, so, AUX_NT);
  }
  if (w == 8) {
    float s = 0.f;
#pragma unroll
    for (int i = 0; i < 8; ++i) s += ((const float*)(lds + L_RH))[i * 32 + (lane & 31)];
    const float u = bfr(bfr(s) + bflo(b1v));
    const float z = 0.7978845608028654f * (u + 0.044715f * u * u * u);
    const float th = 1.0f - 2.0f * __builtin_amdgcn_rcpf(__expf(2.0f * z) + 1.0f);
    const float hv = bfr(0.5f * u * (1.0f + th));
    const int o = (has_mlp && lane < 32) ? WS_HG + ((lane >> 3) * FF + cm * 8 + (lane & 7)) * 8 : OOB;
    __builtin_amdgcn_raw_buffer_store_b64(gpack(hv, tlo, thi), rws, o, 0, AUX_SC1);
  }
  if (w == 0) {
    for (unsigned spins = 0;; ++spins) {
      unsigned bad = 0;
#pragma unroll
      for (int i = 0; i < 4; ++i) bad |= gbad(qg[i][0], qg[i][1], tlo, thi) | gbad(qg[i][2], qg[i][3], tlo, thi);
      if (__builtin_amdgcn_ballot_w64(bad != 0) == 0ull || spins > SPIN_CAP) break;
      __builtin_amdgcn_s_sleep(2);
      asm volatile("" ::: "memory");
#pragma unroll
      for (int i = 0; i < 4; ++i) qg[i] = __builtin_amdgcn_raw_buffer_load_b128(rws, qgo + i * 1024, 0, AUX_SC1);
    }
#pragma unroll
    for (int i = 0; i < 4; ++i)
      ((unsigned*)(lds + L_Q))[i * 64 + lane] = ((qg[i][0] >> 8) & 0xffffu) | ((qg[i][2] << 8) & 0xffff0000u);
    const int so = (xg * NW + 0) * 4096;
#pragma unroll
    for (int t = 0; t < 4; ++t) kr[t] = __builtin_amdgcn_raw_buffer_load_b128(rk, voff + t * 1024, so, AUX_NT);
#pragma unroll
    for (int t = 0; t < 4; ++t) vr[t] = __builtin_amdgcn_raw_buffer_load_b128(rv, voff + t * 1024, so, AUX_NT);
  }
  LDS_BARRIER();
  STAMP(5);

  u32x4 q[4];
#pragma unroll
  for (int g = 0; g < 4; ++g) q[g] = *(const u32x4*)(lds + L_Q + g * 256 + c * 16);
  float m = -INFINITY, l = 0.f, acc[32];
#pragma unroll
  for (int k = 0; k < 32; ++k) acc[k] = 0.f;
  constexpr float SCALE = 0.088388347648318440f, L2E = 1.4426950408889634f;
  unsigned tick = (unsigned)((8 + xg) * NW + w);
  unsigned zoff = 0;
  asm volatile("" : "+v"(zoff));
  GAS unsigned* cnt = (GAS unsigned*)(p.ws + WS_CNT + yg * 256 + zoff);
  bool more = true;
  while (more) {
    float x[16];
#pragma unroll
    for (int t = 0; t < 4; ++t)
#pragma unroll
      for (int g = 0; g < 4; ++g) {
        float d = 0.f;
#pragma unroll
        for (int k = 0; k < 4; ++k) d = dot2(kr[t][k], q[g][k], d);
        x[t * 4 + g] = d;
      }
#pragma unroll
    for (int k = 0; k < 16; k += 8)
      asm volatile("" : "+v"(x[k]), "+v"(x[k + 1]), "+v"(x[k + 2]), "+v"(x[k + 3]), "+v"(x[k + 4]), "+v"(x[k + 5]), "+v"(x[k + 6]), "+v"(x[k + 7]));
    const unsigned nxt = __builtin_amdgcn_readfirstlane(tick);
    more = nxt < (unsigned)PAGES;
    const int so = more ? (int)(nxt * 4096u) : OOB;
    __builtin_amdgcn_sched_barrier(0);
    if (more && lane == 0) tick = __hip_atomic_fetch_add(cnt, 1u, RLX_AGENT);
#pragma unroll
    for (int t = 0; t < 4; ++t) kr[t] = __builtin_amdgcn_raw_buffer_load_b128(rk, voff + t * 1024, so, AUX_NT);
    __builtin_amdgcn_sched_barrier(0);
#pragma unroll
    for (int h = 0; h < 8; ++h) x[h] = trsel<XOR8>(x[h], x[h + 8], b3s);
#pragma unroll
    for (int h = 0; h < 4; ++h) x[h] = trsel<XOR7>(x[h], x[h + 4], b2s);
#pragma unroll
    for (int h = 0; h < 2; ++h) x[h] = trsel<XOR2>(x[h], x[h + 2], b1s);
    x[0] = trsel<XOR1>(x[0], x[1], half != 0);
    const float sc = bfr(bfr(x[0]) * SCALE) * L2E;
    float pm = fmaxf(sc, dpp<ROR4>(sc));
    pm = fmaxf(pm, dpp<XOR8>(pm));
    const float mn = fmaxf(m, pm);
    const float alpha = __builtin_amdgcn_exp2f(m - mn), pr = __builtin_amdgcn_exp2f(sc - mn);
    l = fmaf(l, alpha, pr); m = mn;
    {
      const float a0 = dpp<0x00>(alpha), a1 = dpp<0x55>(alpha), a2 = dpp<0xAA>(alpha), a3 = dpp<0xFF>(alpha);
#pragma unroll
      for (int k = 0; k < 8; ++k) { acc[k] *= a0; acc[8 + k] *= a1; acc[16 + k] *= a2; acc[24 + k] *= a3; }
    }
#define PV(T) { float vf[8]; _Pragma("unroll") for (int k = 0; k < 4; ++k) { vf[2 * k] = bflo(vr[T][k]); vf[2 * k + 1] = bfhi(vr[T][k]); } \
    const float p0 = dpp<NEWBC + 4 * T + 0>(pr), p1 = dpp<NEWBC + 4 * T + 1>(pr), p2 = dpp<NEWBC + 4 * T + 2>(pr), p3 = dpp<NEWBC + 4 * T + 3>(pr); \
    _Pragma("unroll") for (int k = 0; k < 8; ++k) { acc[k] = fmaf(p0, vf[k], acc[k]); acc[8 + k] = fmaf(p1, vf[k], acc[8 + k]); \
      acc[16 + k] = fmaf(p2, vf[k], acc[16 + k]); acc[24 + k] = fmaf(p3, vf[k], acc[24 + k]); } }
    PV(0) PV(1) PV(2) PV(3)
#undef PV
#pragma unroll
    for (int k = 0; k < 32; k += 8)
      asm volatile("" : "+v"(acc[k]), "+v"(acc[k + 1]), "+v"(acc[k + 2]), "+v"(acc[k + 3]), "+v"(acc[k + 4]), "+v"(acc[k + 5]), "+v"(acc[k + 6]), "+v"(acc[k + 7]));
    __builtin_amdgcn_sched_barrier(0);
#pragma unroll
    for (int t = 0; t < 4; ++t) vr[t] = __builtin_amdgcn_raw_buffer_load_b128(rv, voff + t * 1024, so, AUX_NT);
    __builtin_amdgcn_sched_barrier(0);
  }
  STAMP(6);

  {
    l += dpp<ROR4>(l); l += dpp<XOR8>(l);
    const float M = swmax32(swmax16(m));
    const float f = __builtin_amdgcn_exp2f(m - M);
    l *= f;
    l = swadd16(l, l); l = swadd32(l, l);
    const float a0 = dpp<0x00>(f), a1 = dpp<0x55>(f), a2 = dpp<0xAA>(f), a3 = dpp<0xFF>(f);
#pragma unroll
    for (int k = 0; k < 8; ++k) { acc[k] *= a0; acc[8 + k] *= a1; acc[16 + k] *= a2; acc[24 + k] *= a3; }
#pragma unroll
    for (int k = 0; k < 16; ++k) acc[k] = swadd32(acc[k], acc[k + 16]);
#pragma unroll
    for (int k = 0; k < 8; ++k) acc[k] = swadd16(acc[k], acc[k + 8]);
    float* rec = (float*)(lds + L_MRG) + w * 512 + r * 128 + 8 * c;
    *(f32x4*)rec = f32x4{acc[0], acc[1], acc[2], acc[3]};
    *(f32x4*)(rec + 4) = f32x4{acc[4], acc[5], acc[6], acc[7]};
    if (lane < 4) { ((float*)(lds + L_MM))[lane * 16 + w] = M; ((float*)(lds + L_LL))[lane * 16 + w] = l; }
  }
  LDS_BARRIER();
  STAMP(7);
  if (w < 8) {
    const int g = w >> 1, d = tid & 127;
    f32x4 mv[4], lv[4];
#pragma unroll
    for (int i = 0; i < 4; ++i) { mv[i] = *(const f32x4*)(lds + L_MM + g * 64 + i * 16); lv[i] = *(const f32x4*)(lds + L_LL + g * 64 + i * 16); }
    float M = -INFINITY;
#pragma unroll
    for (int i = 0; i < 16; ++i) M = fmaxf(M, mv[i >> 2][i & 3]);
    M = __uint_as_float(__float_as_uint(M) & 0xffffff00u);
    float L = 0.f, o = 0.f;
#pragma unroll
    for (int i = 0; i < 16; ++i) {
      const float wt = __builtin_amdgcn_exp2f(mv[i >> 2][i & 3] - M);
      L = fmaf(wt, lv[i >> 2][i & 3], L);
      o = fmaf(wt, ((const float*)(lds + L_MRG))[i * 512 + tid], o);
    }
    const int rec = (ab * HQ + akv * 4 + g) * 8 + xg;
    __builtin_amdgcn_raw_buffer_store_b64(gpack(o, tlo, thi), rws, WS_PG + (rec * 128 + d) * 8, 0, AUX_SC1);
    u32x2 ml = gpack(d == 0 ? M : L, tlo, thi);
    if (d == 0) ml[0] = (__float_as_uint(M) >> 8) | tlo;
    __builtin_amdgcn_raw_buffer_store_b64(ml, rws, d < 2 ? WS_ML + (rec * 2 + d) * 8 : OOB, 0, AUX_SC1);
    STAMP(8);

    const int tb = w >> 1, kh = w & 1, sp = lane >> 3, dd = lane & 7;
    const int crec = (tb * HQ + oh) * 8 + sp;
    const int po = WS_PG + (crec * 128 + kh * 64) * 8 + dd * 16, mo = WS_ML + crec * 16;
    u32x4 pg[4], mg;
    mg = __builtin_amdgcn_raw_buffer_load_b128(rws, mo, 0, AUX_SC1);
#pragma unroll
    for (int i = 0; i < 4; ++i) pg[i] = __builtin_amdgcn_raw_buffer_load_b128(rws, po + i * 128, 0, AUX_SC1);
    __builtin_amdgcn_sched_barrier(0);
    u32x4 wc[8];
#pragma unroll
    for (int i = 0; i < 8; ++i) wc[i] = *(const u32x4*)(lds + L_WOT + lane * 272 + kh * 128 + i * 16);
    for (unsigned spins = 0;; ++spins) {
      unsigned bad = gbad(mg[0], mg[1], tlo, thi) | gbad(mg[2], mg[3], tlo, thi);
#pragma unroll
      for (int i = 0; i < 4; ++i) bad |= gbad(pg[i][0], pg[i][1], tlo, thi) | gbad(pg[i][2], pg[i][3], tlo, thi);
      if (__builtin_amdgcn_ballot_w64(bad != 0) == 0ull || spins > SPIN_CAP) break;
      __builtin_amdgcn_s_sleep(4);
      asm volatile("" ::: "memory");
      if (bad) {
        mg = __builtin_amdgcn_raw_buffer_load_b128(rws, mo, 0, AUX_SC1);
#pragma unroll
        for (int i = 0; i < 4; ++i) pg[i] = __builtin_amdgcn_raw_buffer_load_b128(rws, po + i * 128, 0, AUX_SC1);
      }
    }
    STAMP(9);
    const float ms = gval(mg[0]), ls = gval(mg[2]);
    float Mx = fmaxf(ms, dpp<XOR8>(ms));
    Mx = swmax32(swmax16(Mx));
    const float wt = __builtin_amdgcn_exp2f(ms - Mx);
    float Ls = wt * ls;
    Ls += dpp<XOR8>(Ls); Ls = swadd16(Ls, Ls); Ls = swadd32(Ls, Ls);
    float v[8];
#pragma unroll
    for (int i = 0; i < 4; ++i) { v[2 * i] = wt * gval(pg[i][0]); v[2 * i + 1] = wt * gval(pg[i][2]); }
#pragma unroll
    for (int i = 0; i < 4; ++i) v[i] = swadd32(v[i], v[i + 4]);
#pragma unroll
    for (int i = 0; i < 2; ++i) v[i] = swadd16(v[i], v[i + 2]);
    v[0] = trsel<XOR8>(v[0], v[1], b3s);
    const int dim = 2 * dd + ((lane >> 3) & 1) + 16 * (lane >> 4);
    u16* ob = (u16*)(lds + L_O + w * 128);
    ob[dim] = (u16)bfbits(v[0] * __builtin_amdgcn_rcpf(Ls));
    asm volatile("s_waitcnt lgkmcnt(0)" ::: "memory");
    float s = 0.f;
#pragma unroll
    for (int i = 0; i < 8; ++i) {
      const u32x4 ov = *(const u32x4*)(lds + L_O + w * 128 + i * 16);
#pragma unroll
      for (int k = 0; k < 4; ++k) s = dot2(wc[i][k], ov[k], s);
    }
    __builtin_amdgcn_raw_buffer_store_b64(gpack(s, tlo, thi), rws, WS_SG + (((oh * 2 + kh) * NB + tb) * DM + onc * 64 + lane) * 8, 0, AUX_SC1);
    STAMP(10);
  } else if (w < 12) {
    const int tb = w - 8;
    const int ho = lane < 56 ? WS_HG + (tb * FF + oh * 56 + lane) * 8 : WS_HG + (tb * FF + oh * 56) * 8;
    u32x2 hg = __builtin_amdgcn_raw_buffer_load_b64(rws, ho, 0, AUX_SC1);
    u32x4 wc[7];
#pragma unroll
    for (int i = 0; i < 7; ++i) wc[i] = *(const u32x4*)(lds + L_W2T + lane * 144 + i * 16);
    for (unsigned spins = 0;; ++spins) {
      const unsigned bad = gbad(hg[0], hg[1], tlo, thi);
      if (__builtin_amdgcn_ballot_w64(bad != 0) == 0ull || spins > SPIN_CAP) break;
      __builtin_amdgcn_s_sleep(4);
      asm volatile("" ::: "memory");
      hg = __builtin_amdgcn_raw_buffer_load_b64(rws, ho, 0, AUX_SC1);
    }
    u16* ob = (u16*)(lds + L_O + w * 128);
    ob[lane] = (u16)((hg[0] >> 8) & 0xffffu);
    asm volatile("s_waitcnt lgkmcnt(0)" ::: "memory");
    float s = 0.f;
#pragma unroll
    for (int i = 0; i < 7; ++i) {
      const u32x4 ov = *(const u32x4*)(lds + L_O + w * 128 + i * 16);
#pragma unroll
      for (int k = 0; k < 4; ++k) s = dot2(wc[i][k], ov[k], s);
    }
    __builtin_amdgcn_raw_buffer_store_b64(gpack(s, tlo, thi), rws, WS_MG + ((oh * NB + tb) * DM + onc * 64 + lane) * 8, 0, AUX_SC1);
  } else if (w == 12) {
    const int fb = bid >> 6, n0 = (bid & 63) * 8, kq = lane >> 2, p2 = lane & 3;
    const int so = WS_SG + ((kq * NB + fb) * DM + n0 + 2 * p2) * 8, mo = WS_MG + ((kq * NB + fb) * DM + n0 + 2 * p2) * 8;
    constexpr int KSTR = 16 * NB * DM * 8;
    const __amdgpu_buffer_rsrc_t rb = __builtin_amdgcn_make_buffer_rsrc((void*)p.b2, 0, DM * 2, RSRC_W3);
    const unsigned b2v = __builtin_amdgcn_raw_buffer_load_b32(rb, (n0 + 2 * p2) * 2, 0, 0);
    const unsigned xv = __builtin_amdgcn_raw_buffer_load_b32(rx, (fb * DM + n0 + 2 * p2) * 2, 0, 0);
    u32x4 sg[4], mg[2];
    for (unsigned spins = 0;; ++spins) {
#pragma unroll
      for (int i = 0; i < 2; ++i) mg[i] = __builtin_amdgcn_raw_buffer_load_b128(rws, mo + i * KSTR, 0, AUX_SC1);
#pragma unroll
      for (int i = 0; i < 4; ++i) sg[i] = __builtin_amdgcn_raw_buffer_load_b128(rws, so + i * KSTR, 0, AUX_SC1);
      unsigned bad = 0;
#pragma unroll
      for (int i = 0; i < 2; ++i) bad |= gbad(mg[i][0], mg[i][1], tlo, thi) | gbad(mg[i][2], mg[i][3], tlo, thi);
#pragma unroll
      for (int i = 0; i < 4; ++i) bad |= gbad(sg[i][0], sg[i][1], tlo, thi) | gbad(sg[i][2], sg[i][3], tlo, thi);
      if (__builtin_amdgcn_ballot_w64(bad != 0) == 0ull || spins > SPIN_CAP) break;
      __builtin_amdgcn_s_sleep(4);
      asm volatile("" ::: "memory");
    }
    STAMP(11);
    float a[4];
    a[0] = (gval(sg[0][0]) + gval(sg[1][0])) + (gval(sg[2][0]) + gval(sg[3][0]));
    a[1] = (gval(sg[0][2]) + gval(sg[1][2])) + (gval(sg[2][2]) + gval(sg[3][2]));
    a[2] = gval(mg[0][0]) + gval(mg[1][0]);
    a[3] = gval(mg[0][2]) + gval(mg[1][2]);
#pragma unroll
    for (int i = 0; i < 4; ++i) {
      float t = a[i];
      t += dpp<XOR8>(t); t += dpp<ROR4>(t);
      t = swadd16(t, t); t = swadd32(t, t);
      a[i] = t;
    }
    float o[2];
#pragma unroll
    for (int j = 0; j < 2; ++j) {
      const float s = bfr(a[j]);
      const float mm = bfr(bfr(a[2 + j]) + (j ? bfhi(b2v) : bflo(b2v)));
      const float g = bfr(mm * __builtin_amdgcn_rcpf(1.0f + __expf(-mm)));
      o[j] = (j ? bfhi(xv) : bflo(xv)) + bfr(s * g);
    }
    if (lane < 4) *(unsigned*)(p.out + fb * DM + n0 + 2 * p2) = bfbits(o[0]) | (bfbits(o[1]) << 16);
    if (bid == 0 && lane == 0) __hip_atomic_store((GAS u64*)(p.ws + WS_EPOCH), epoch + 1ull, RLX_AGENT);
    STAMP(12);
  }
}

extern "C" void kernel_launch(void* const* d_in, const int* in_sizes, int n_in,
                              void* d_out, int out_size, void* d_ws, size_t ws_size,
                              hipStream_t stream) {
  Params p;
  p.x = (const u16*)d_in[0]; p.kc = (const u16*)d_in[1]; p.vc = (const u16*)d_in[2];
  p.wq = (const u16*)d_in[3]; p.wo = (const u16*)d_in[4];
  p.w1 = (const u16*)d_in[6]; p.b1 = (const u16*)d_in[7]; p.w2 = (const u16*)d_in[8]; p.b2 = (const u16*)d_in[9];
  p.out = (u16*)d_out; p.ws = (unsigned char*)d_ws;
  hipLaunchKernelGGL(fused_decode_kernel, dim3(NWG), dim3(NT), 0, stream, p);
}
```

```cpp
#include <hip/hip_runtime.h>
#include <hip/hip_bf16.h>
#include <stdint.h>

#define DYN 0

typedef unsigned u32x4 __attribute__((ext_vector_type(4)));
typedef unsigned u32x2 __attribute__((ext_vector_type(2)));
typedef float f32x4 __attribute__((ext_vector_type(4)));
typedef __bf16 bf16x2 __attribute__((ext_vector_type(2)));
typedef unsigned short u16;
typedef unsigned long long u64;
#define GAS __attribute__((address_space(1)))
#define RLX_AGENT __ATOMIC_RELAXED, __HIP_MEMORY_SCOPE_AGENT

constexpr int NB = 4, DM = 512, HQ = 32, HKV = 8, SK = 16384, DH = 128, FF = 1792;
constexpr int NWG = 256, NW = 16, NT = NW * 64;
constexpr int PAGES = SK / 16;
constexpr int AUX_NT = 2, AUX_SC1 = 16;
constexpr int RSRC_W3 = 0x00020000;
constexpr int WS_EPOCH = 0;
constexpr int WS_CNT = 4096;
constexpr int WS_QG = 16384;
constexpr int WS_HG = WS_QG + NB * 4096 * 8;
constexpr int WS_ML = WS_HG + NB * FF * 8;
constexpr int WS_PG = WS_ML + NB * HQ * 8 * 2 * 8;
constexpr int WS_SG = WS_PG + NB * HQ * 8 * 128 * 8;
constexpr int WS_MG = WS_SG + 64 * NB * DM * 8;
constexpr int WS_END = WS_MG + 32 * NB * DM * 8;
constexpr int WS_STAMP = 8 << 20;
constexpr int OOB = 0x7ff00000;
constexpr int L_PARK = 0;
constexpr int L_WOT = L_PARK + 8 * 8192;
constexpr int L_W2T = L_WOT + 64 * 272;
constexpr int L_MRG = L_W2T + 64 * 144;
constexpr int L_MM = L_MRG + NW * 512 * 4;
constexpr int L_LL = L_MM + 256;
constexpr int L_RQ = L_LL + 256;
constexpr int L_RH = L_RQ + 8 * 64 * 4;
constexpr int L_Q = L_RH + 4 * 32 * 4;
constexpr int L_O = L_Q + 1024;
constexpr int L_XN = L_O + 16 * 128;
constexpr int L_Y = L_XN + 4096;
constexpr int L_TAG = L_Y + 4096;
constexpr int L_END = L_TAG + 16;
#define GDELAY 24
constexpr unsigned SPIN_CAP = 1u << 16;

struct Params {
  const u16* x; const u16* kc; const u16* vc; const u16* wq; const u16* wo;
  const u16* w1; const u16* b1; const u16* w2; const u16* b2;
  u16* out; unsigned char* ws;
};

__device__ __forceinline__ float dot2(unsigned a, unsigned b, float acc) {
  return __builtin_amdgcn_fdot2_f32_bf16(__builtin_bit_cast(bf16x2, a), __builtin_bit_cast(bf16x2, b), acc, false);
}
template <int CTRL> __device__ __forceinline__ float dpp(float x) {
  return __builtin_bit_cast(float, __builtin_amdgcn_mov_dpp(__builtin_bit_cast(int, x), CTRL, 0xf, 0xf, true));
}
constexpr int XOR1 = 0xB1, XOR2 = 0x4E, XOR7 = 0x141, XOR8 = 0x128, ROR4 = 0x124, NEWBC = 0x150;
__device__ __forceinline__ float bfr(float x) { return (float)(__bf16)x; }
__device__ __forceinline__ unsigned bfbits(float x) { return (unsigned)__builtin_bit_cast(u16, (__bf16)x); }
__device__ __forceinline__ float bflo(unsigned u) { return __builtin_bit_cast(float, u << 16); }
__device__ __forceinline__ float bfhi(unsigned u) { return __builtin_bit_cast(float, u & 0xffff0000u); }
__device__ __forceinline__ float swadd32(float a, float b) {
  auto s = __builtin_amdgcn_permlane32_swap(__float_as_uint(a), __float_as_uint(b), false, false);
  return __uint_as_float(s[0]) + __uint_as_float(s[1]);
}
__device__ __forceinline__ float swadd16(float a, float b) {
  auto s = __builtin_amdgcn_permlane16_swap(__float_as_uint(a), __float_as_uint(b), false, false);
  return __uint_as_float(s[0]) + __uint_as_float(s[1]);
}
__device__ __forceinline__ float swmax32(float a) {
  auto s = __builtin_amdgcn_permlane32_swap(__float_as_uint(a), __float_as_uint(a), false, false);
  return fmaxf(__uint_as_float(s[0]), __uint_as_float(s[1]));
}
__device__ __forceinline__ float swmax16(float a) {
  auto s = __builtin_amdgcn_permlane16_swap(__float_as_uint(a), __float_as_uint(a), false, false);
  return fmaxf(__uint_as_float(s[0]), __uint_as_float(s[1]));
}
__device__ __forceinline__ float wave_sum(float v) {
  v += dpp<XOR1>(v); v += dpp<XOR2>(v); v += dpp<XOR7>(v); v += dpp<XOR8>(v);
  v = swadd16(v, v); v = swadd32(v, v);
  return v;
}
template <int CTRL> __device__ __forceinline__ float trsel(float lo, float hi, bool sel) {
  const float t1 = lo + dpp<CTRL>(lo), t2 = hi + dpp<CTRL>(hi);
  return sel ? t2 : t1;
}
__device__ __forceinline__ float divr(float a, float d, float r) {
  const float q = a * r;
  return fmaf(fmaf(-q, d, a), r, q);
}
__device__ __forceinline__ u32x2 gpack(float v, unsigned tlo, unsigned thi) {
  const unsigned b = __float_as_uint(v) + 0x80u;
  return u32x2{(b >> 8) | tlo, thi};
}
__device__ __forceinline__ unsigned gbad(unsigned lo, unsigned hi, unsigned tlo, unsigned thi) {
  return (hi ^ thi) | ((lo ^ tlo) & 0xff000000u);
}
__device__ __forceinline__ float gval(unsigned lo) { return __uint_as_float(lo << 8); }
#define LDS_BARRIER() do { asm volatile("s_waitcnt lgkmcnt(0)" ::: "memory"); __builtin_amdgcn_s_barrier(); asm volatile("" ::: "memory"); } while (0)

#define STAMP(i) do {} while (0)

__device__ __forceinline__ void layernorm8(float (&v)[8]) {
  const float s = ((v[0] + v[1]) + (v[2] + v[3])) + ((v[4] + v[5]) + (v[6] + v[7]));
  const float mean = wave_sum(s) * (1.0f / DM);
  float q0 = 0.f, q1 = 0.f;
#pragma unroll
  for (int i = 0; i < 8; i += 2) { const float d0 = v[i] - mean, d1 = v[i + 1] - mean; q0 = fmaf(d0, d0, q0); q1 = fmaf(d1, d1, q1); }
  const float var = wave_sum(q0 + q1) * (1.0f / DM);
  const float mb = bfr(mean);
  const float den = bfr(__builtin_amdgcn_sqrtf(bfr(bfr(var) + 1e-5f)));
  const float rc = __builtin_amdgcn_rcpf(den);
#pragma unroll
  for (int i = 0; i < 8; ++i) v[i] = bfr(divr(bfr(v[i] - mb), den, rc));
}
__device__ __forceinline__ u32x4 pack8(const float (&v)[8]) {
  u32x4 o;
#pragma unroll
  for (int i = 0; i < 4; ++i) o[i] = (__float_as_uint(v[2 * i]) >> 16) | (__float_as_uint(v[2 * i + 1]) & 0xffff0000u);
  return o;
}
#define PIN8(a, k) asm volatile("" : "+v"(a[k]), "+v"(a[k + 1]), "+v"(a[k + 2]), "+v"(a[k + 3]), "+v"(a[k + 4]), "+v"(a[k + 5]), "+v"(a[k + 6]), "+v"(a[k + 7]))

__device__ __forceinline__ float softmax_step(float (&x)[16], float& m, float& l, float (&acc)[32], bool b0s, bool b1s, bool b2s, bool b3s) {
  constexpr float SCALE = 0.088388347648318440f, L2E = 1.4426950408889634f;
#pragma unroll
  for (int h = 0; h < 8; ++h) x[h] = trsel<XOR8>(x[h], x[h + 8], b3s);
#pragma unroll
  for (int h = 0; h < 4; ++h) x[h] = trsel<XOR7>(x[h], x[h + 4], b2s);
#pragma unroll
  for (int h = 0; h < 2; ++h) x[h] = trsel<XOR2>(x[h], x[h + 2], b1s);
  x[0] = trsel<XOR1>(x[0], x[1], b0s);
  const float sc = bfr(bfr(x[0]) * SCALE) * L2E;
  float pm = fmaxf(sc, dpp<ROR4>(sc));
  pm = fmaxf(pm, dpp<XOR8>(pm));
  const float mn = fmaxf(m, pm);
  const float alpha = __builtin_amdgcn_exp2f(m - mn), pr = __builtin_amdgcn_exp2f(sc - mn);
  l = fmaf(l, alpha, pr); m = mn;
  const float a0 = dpp<0x00>(alpha), a1 = dpp<0x55>(alpha), a2 = dpp<0xAA>(alpha), a3 = dpp<0xFF>(alpha);
#pragma unroll
  for (int k = 0; k < 8; ++k) { acc[k] *= a0; acc[8 + k] *= a1; acc[16 + k] *= a2; acc[24 + k] *= a3; }
  return pr;
}
template <int T> __device__ __forceinline__ void pv_step(const u32x4& v, float pr, float (&acc)[32]) {
  float vf[8];
#pragma unroll
  for (int k = 0; k < 4; ++k) { vf[2 * k] = bflo(v[k]); vf[2 * k + 1] = bfhi(v[k]); }
  const float p0 = dpp<NEWBC + 4 * T + 0>(pr), p1 = dpp<NEWBC + 4 * T + 1>(pr), p2 = dpp<NEWBC + 4 * T + 2>(pr), p3 = dpp<NEWBC + 4 * T + 3>(pr);
#pragma unroll
  for (int k = 0; k < 8; ++k) {
    acc[k] = fmaf(p0, vf[k], acc[k]); acc[8 + k] = fmaf(p1, vf[k], acc[8 + k]);
    acc[16 + k] = fmaf(p2, vf[k], acc[16 + k]); acc[24 + k] = fmaf(p3, vf[k], acc[24 + k]);
  }
}
__device__ __forceinline__ void qk4(const u32x4& k, const u32x4 (&q)[4], float* x) {
#pragma unroll
  for (int g = 0; g < 4; ++g) {
    float d = 0.f;
#pragma unroll
    for (int i = 0; i < 4; ++i) d = dot2(k[i], q[g][i], d);
    x[g] = d;
  }
}

__global__ void __launch_bounds__(NT, 4) fused_decode_kernel(Params p) {
  __shared__ __attribute__((aligned(16))) unsigned char lds[L_END];
  const int bid = blockIdx.x, tid = threadIdx.x, lane = tid & 63;
  const int w = __builtin_amdgcn_readfirstlane(tid >> 6);
  const int r = lane >> 4, c = lane & 15;
  const int xg = bid & 7, yg = bid >> 3;
  STAMP(0);
  const __amdgpu_buffer_rsrc_t rws = __builtin_amdgcn_make_buffer_rsrc((void*)p.ws, 0, WS_END, RSRC_W3);
  const __amdgpu_buffer_rsrc_t rx = __builtin_amdgcn_make_buffer_rsrc((void*)p.x, 0, NB * DM * 2, RSRC_W3);
  const int ab = yg >> 3, akv = yg & 7;
  const size_t kvbase = (size_t)(ab * HKV + akv) * SK * DH;
  const __amdgpu_buffer_rsrc_t rk = __builtin_amdgcn_make_buffer_rsrc((void*)(p.kc + kvbase), 0, PAGES * 4096, RSRC_W3);
  const __amdgpu_buffer_rsrc_t rv = __builtin_amdgcn_make_buffer_rsrc((void*)(p.vc + kvbase), 0, PAGES * 4096, RSRC_W3);
  const int voff = lane * 16;
  const int pg0 = xg * NW + w;
  const bool b0s = lane & 1, b1s = lane & 2, b2s = lane & 4, b3s = lane & 8;
  const int qj = xg * 32 + yg;
  const bool has_mlp = yg < 28;
  const int cm = xg * 28 + yg;
  const int oh = yg, onc = xg;
#define LOAD_PAGE(so_) do { \
    _Pragma("unroll") for (int t = 0; t < 4; ++t) kr[t] = __builtin_amdgcn_raw_buffer_load_b128(rk, voff + t * 1024, (so_), AUX_NT); \
    _Pragma("unroll") for (int t = 0; t < 4; ++t) vr[t] = __builtin_amdgcn_raw_buffer_load_b128(rv, voff + t * 1024, (so_), AUX_NT); } while (0)

  u32x4 kr[4], vr[4];
  unsigned tlo, thi;
  u64 epoch;
#define READ_TAG() do { const u32x2 ep_ = *(const u32x2*)(lds + L_TAG); \
    epoch = ((u64)__builtin_amdgcn_readfirstlane(ep_[1]) << 32) | (u64)__builtin_amdgcn_readfirstlane(ep_[0]); \
    const u64 tg_ = ((epoch + 1ull) * 0x9E3779B97F4A7C15ull) >> 24; tlo = (unsigned)(tg_ & 0xffu) << 24; thi = (unsigned)(tg_ >> 8); } while (0)
#define RAW_BARRIER() do { __builtin_amdgcn_sched_barrier(0); __builtin_amdgcn_s_barrier(); __builtin_amdgcn_sched_barrier(0); } while (0)
  if (w < 4) {
    const u32x4 xa = __builtin_amdgcn_raw_buffer_load_b128(rx, (w * DM + lane * 8) * 2, 0, 0);
    const u32x2 e = __builtin_amdgcn_raw_buffer_load_b64(rws, w == 0 ? WS_EPOCH : OOB, 0, AUX_SC1);
    const __amdgpu_buffer_rsrc_t rb = __builtin_amdgcn_make_buffer_rsrc((void*)p.b1, 0, FF * 2, RSRC_W3);
    const unsigned bb = __builtin_amdgcn_raw_buffer_load_b16(rb, (has_mlp && w == 1) ? (cm * 8 + (lane & 7)) * 2 : OOB, 0, 0);
    RAW_BARRIER();
    STAMP(1);
    float lnv[8];
#pragma unroll
    for (int i = 0; i < 4; ++i) { lnv[2 * i] = bflo(xa[i]); lnv[2 * i + 1] = bfhi(xa[i]); }
    layernorm8(lnv);
    *(u32x4*)(lds + L_XN + (w * DM + lane * 8) * 2) = pack8(lnv);
    if (w == 0 && lane == 0) *(u32x2*)(lds + L_TAG) = e;
    LDS_BARRIER();
    STAMP(2);
    layernorm8(lnv);
    *(u32x4*)(lds + L_Y + (w * DM + lane * 8) * 2) = pack8(lnv);
    READ_TAG();
    LDS_BARRIER();
    STAMP(3);
    if (w == 0) {
      float sq = 0.f;
#pragma unroll
      for (int i = 0; i < 8; ++i) sq += ((const float*)(lds + L_RQ))[i * 64 + lane];
      __builtin_amdgcn_raw_buffer_store_b64(gpack(bfr(sq), tlo, thi), rws, WS_QG + (r * 4096 + qj * 16 + c) * 8, 0, AUX_SC1);
      STAMP(4);
      __builtin_amdgcn_s_sleep(GDELAY);
      asm volatile("" ::: "memory");
      u32x4 qg[4];
      const int qgo = WS_QG + (ab * 4096 + akv * 512) * 8 + lane * 16;
#pragma unroll
      for (int i = 0; i < 4; ++i) qg[i] = __builtin_amdgcn_raw_buffer_load_b128(rws, qgo + i * 1024, 0, AUX_SC1);
      RAW_BARRIER();
      STAMP(5);
      for (unsigned spins = 0;; ++spins) {
        unsigned bad = 0;
#pragma unroll
        for (int i = 0; i < 4; ++i) bad |= gbad(qg[i][0], qg[i][1], tlo, thi) | gbad(qg[i][2], qg[i][3], tlo, thi);
        if (__builtin_amdgcn_ballot_w64(bad != 0) == 0ull || spins > SPIN_CAP) break;
        STAMP(13);
        asm volatile("" ::: "memory");
#pragma unroll
        for (int i = 0; i < 4; ++i) qg[i] = __builtin_amdgcn_raw_buffer_load_b128(rws, qgo + i * 1024, 0, AUX_SC1);
      }
#pragma unroll
      for (int i = 0; i < 4; ++i)
        ((unsigned*)(lds + L_Q))[i * 64 + lane] = ((qg[i][0] >> 8) & 0xffffu) | ((qg[i][2] << 8) & 0xffff0000u);
    } else {
      RAW_BARRIER();
      asm volatile("" ::: "memory");
      STAMP(5);
      float sh = 0.f;
#pragma unroll
      for (int i = 0; i < 4; ++i) sh += ((const float*)(lds + L_RH))[i * 32 + (lane & 31)];
      const float u = bfr(bfr(sh) + bflo(bb));
      const float z = 0.7978845608028654f * (u + 0.044715f * u * u * u);
      const float th = 1.0f - 2.0f * __builtin_amdgcn_rcpf(__expf(2.0f * z) + 1.0f);
      const float hv = bfr(0.5f * u * (1.0f + th));
      const int o = (has_mlp && w == 1 && lane < 32) ? WS_HG + ((lane >> 3) * FF + cm * 8 + (lane & 7)) * 8 : OOB;
      __builtin_amdgcn_raw_buffer_store_b64(gpack(hv, tlo, thi), rws, o, 0, AUX_SC1);
    }
    LOAD_PAGE(pg0 * 4096);
  } else if (w < 12) {
    const int arow = (w - 4) * 64 + (lane >> 1) * 2;
    const __amdgpu_buffer_rsrc_t rq = __builtin_amdgcn_make_buffer_rsrc((void*)p.wq, 0, DM * 4096 * 2, RSRC_W3);
    const int o = (arow * 4096 + qj * 16 + (lane & 1) * 8) * 2;
    const u32x4 wa = __builtin_amdgcn_raw_buffer_load_b128(rq, o, 0, 0);
    const u32x4 wb = __builtin_amdgcn_raw_buffer_load_b128(rq, o + 8192, 0, 0);
    RAW_BARRIER();
    LOAD_PAGE(pg0 * 4096);
    __builtin_amdgcn_sched_barrier(0);
    STAMP(1);
    LDS_BARRIER();
    STAMP(2);
    {
      unsigned apk[4];
#pragma unroll
      for (int b = 0; b < 4; ++b) apk[b] = *(const unsigned*)(lds + L_XN + (b * DM + arow) * 2);
      float y[32];
#pragma unroll
      for (int k = 0; k < 4; ++k) {
        const unsigned lo = __builtin_amdgcn_perm(wb[k], wa[k], 0x05040100u), hi = __builtin_amdgcn_perm(wb[k], wa[k], 0x07060302u);
#pragma unroll
        for (int b = 0; b < 4; ++b) { y[b * 8 + 2 * k] = dot2(lo, apk[b], 0.f); y[b * 8 + 2 * k + 1] = dot2(hi, apk[b], 0.f); }
      }
#pragma unroll
      for (int h = 0; h < 16; ++h) y[h] = swadd32(y[h], y[h + 16]);
#pragma unroll
      for (int h = 0; h < 8; ++h) y[h] = swadd16(y[h], y[h + 8]);
#pragma unroll
      for (int h = 0; h < 4; ++h) y[h] = trsel<ROR4>(y[h], y[h + 4], b2s);
#pragma unroll
      for (int h = 0; h < 2; ++h) y[h] = trsel<XOR8>(y[h], y[h + 2], b3s);
      y[0] = trsel<XOR2>(y[0], y[1], b1s);
      const int col = (b0s ? 8 : 0) + (b2s ? 4 : 0) + (b3s ? 2 : 0) + (b1s ? 1 : 0);
      ((float*)(lds + L_RQ))[(w - 4) * 64 + r * 16 + col] = y[0];
    }
    READ_TAG();
    LDS_BARRIER();
    STAMP(3);
    RAW_BARRIER();
    STAMP(5);
    const int t2 = tid - 256, kp = t2 >> 3, cn = t2 & 7;
    const __amdgpu_buffer_rsrc_t ro = __builtin_amdgcn_make_buffer_rsrc((void*)p.wo, 0, 4096 * DM * 2, RSRC_W3);
    const int oo = ((oh * 128 + 2 * kp) * DM + onc * 64 + cn * 8) * 2;
    const u32x4 ua = __builtin_amdgcn_raw_buffer_load_b128(ro, oo, 0, AUX_NT);
    const u32x4 ub = __builtin_amdgcn_raw_buffer_load_b128(ro, oo + DM * 2, 0, AUX_NT);
    __builtin_amdgcn_sched_barrier(0);
    unsigned char* pk = lds + L_PARK + (w - 4) * 8192 + voff;
#pragma unroll
    for (int t = 0; t < 4; ++t) *(u32x4*)(pk + t * 1024) = kr[t];
#pragma unroll
    for (int t = 0; t < 4; ++t) *(u32x4*)(pk + 4096 + t * 1024) = vr[t];
    __builtin_amdgcn_sched_barrier(0);
    LOAD_PAGE((pg0 + 128) * 4096);
    __builtin_amdgcn_sched_barrier(0);
    unsigned char* dst = lds + L_WOT + cn * 272 + kp * 4;
#pragma unroll
    for (int k = 0; k < 4; ++k) {
      *(unsigned*)(dst + (2 * k) * 8 * 272) = __builtin_amdgcn_perm(ub[k], ua[k], 0x05040100u);
      *(unsigned*)(dst + (2 * k + 1) * 8 * 272) = __builtin_amdgcn_perm(ub[k], ua[k], 0x07060302u);
    }
  } else {
    const int arow = (w - 12) * 128 + lane * 2;
    const __amdgpu_buffer_rsrc_t r1s = __builtin_amdgcn_make_buffer_rsrc((void*)p.w1, 0, DM * FF * 2, RSRC_W3);
    const int o = has_mlp ? (arow * FF + cm * 8) * 2 : OOB;
    const u32x4 wa = __builtin_amdgcn_raw_buffer_load_b128(r1s, o, 0, 0);
    const u32x4 wb = __builtin_amdgcn_raw_buffer_load_b128(r1s, o + FF * 2, 0, 0);
    RAW_BARRIER();
    STAMP(1);
    LDS_BARRIER();
    STAMP(2);
    READ_TAG();
    LDS_BARRIER();
    STAMP(3);
    {
      unsigned apk[4];
#pragma unroll
      for (int b = 0; b < 4; ++b) apk[b] = *(const unsigned*)(lds + L_Y + (b * DM + arow) * 2);
      float y[32];
#pragma unroll
      for (int k = 0; k < 4; ++k) {
        const unsigned lo = __builtin_amdgcn_perm(wb[k], wa[k], 0x05040100u), hi = __builtin_amdgcn_perm(wb[k], wa[k], 0x07060302u);
#pragma unroll
        for (int b = 0; b < 4; ++b) { y[b * 8 + 2 * k] = dot2(lo, apk[b], 0.f); y[b * 8 + 2 * k + 1] = dot2(hi, apk[b], 0.f); }
      }
#pragma unroll
      for (int h = 0; h < 16; ++h) y[h] = swadd32(y[h], y[h + 16]);
#pragma unroll
      for (int h = 0; h < 8; ++h) y[h] = swadd16(y[h], y[h + 8]);
#pragma unroll
      for (int h = 0; h < 4; ++h) y[h] = trsel<XOR8>(y[h], y[h + 4], b3s);
#pragma unroll
      for (int h = 0; h < 2; ++h) y[h] = trsel<XOR7>(y[h], y[h + 2], b2s);
      y[0] = trsel<XOR2>(y[0], y[1], b1s);
      y[0] += dpp<XOR1>(y[0]);
      const int col = (b3s ? 4 : 0) + (b2s ? 2 : 0) + (b1s ? 1 : 0);
      if (!b0s) ((float*)(lds + L_RH))[(w - 12) * 32 + r * 8 + col] = y[0];
    }
    __builtin_amdgcn_sched_barrier(0);
    LDS_BARRIER();
    __builtin_amdgcn_sched_barrier(0);
    STAMP(5);
    const int t2 = tid - 768, kp = t2 >> 3, cn = t2 & 7;
    const __amdgpu_buffer_rsrc_t r2 = __builtin_amdgcn_make_buffer_rsrc((void*)p.w2, 0, FF * DM * 2, RSRC_W3);
    const int oo = kp < 28 ? ((oh * 56 + 2 * kp) * DM + onc * 64 + cn * 8) * 2 : OOB;
    const u32x4 ua = __builtin_amdgcn_raw_buffer_load_b128(r2, oo, 0, AUX_NT);
    const u32x4 ub = __builtin_amdgcn_raw_buffer_load_b128(r2, oo + DM * 2, 0, AUX_NT);
    LOAD_PAGE(pg0 * 4096);
    __builtin_amdgcn_sched_barrier(0);
    unsigned char* dst = lds + L_W2T + cn * 144 + kp * 4;
#pragma unroll
    for (int k = 0; k < 4; ++k) {
      *(unsigned*)(dst + (2 * k) * 8 * 144) = __builtin_amdgcn_perm(ub[k], ua[k], 0x05040100u);
      *(unsigned*)(dst + (2 * k + 1) * 8 * 144) = __builtin_amdgcn_perm(ub[k], ua[k], 0x07060302u);
    }
  }
  LDS_BARRIER();
  STAMP(6);

  u32x4 q[4];
#pragma unroll
  for (int g = 0; g < 4; ++g) q[g] = *(const u32x4*)(lds + L_Q + g * 256 + c * 16);
  float m = -INFINITY, l = 0.f, acc[32];
#pragma unroll
  for (int k = 0; k < 32; ++k) acc[k] = 0.f;
  const bool early = w >= 4 && w < 12;
  unsigned tick = (unsigned)(pg0 + (early ? 256 : 128));
  bool more = true;
  unsigned parkat = early ? tick : 0xffffffffu;
  asm volatile("" : "+v"(parkat));
  parkat = __builtin_amdgcn_readfirstlane(parkat);
  while (more) {
    float x[16];
#pragma unroll
    for (int t = 0; t < 4; ++t) qk4(kr[t], q, x + 4 * t);
    PIN8(x, 0); PIN8(x, 8);
    const unsigned nxt = __builtin_amdgcn_readfirstlane(tick);
    more = nxt < (unsigned)PAGES;
    const int so = more ? (int)(nxt * 4096u) : OOB;
    __builtin_amdgcn_sched_barrier(0);
    tick = nxt + 8 * NW;
#pragma unroll
    for (int t = 0; t < 4; ++t) kr[t] = __builtin_amdgcn_raw_buffer_load_b128(rk, voff + t * 1024, so, AUX_NT);
    __builtin_amdgcn_sched_barrier(0);
    const float pr = softmax_step(x, m, l, acc, b0s, b1s, b2s, b3s);
    pv_step<0>(vr[0], pr, acc); pv_step<1>(vr[1], pr, acc); pv_step<2>(vr[2], pr, acc); pv_step<3>(vr[3], pr, acc);
    PIN8(acc, 0); PIN8(acc, 8); PIN8(acc, 16); PIN8(acc, 24);
    __builtin_amdgcn_sched_barrier(0);
#pragma unroll
    for (int t = 0; t < 4; ++t) vr[t] = __builtin_amdgcn_raw_buffer_load_b128(rv, voff + t * 1024, so, AUX_NT);
    __builtin_amdgcn_sched_barrier(0);
    if (nxt == parkat) {
      const unsigned char* pk = lds + L_PARK + (w - 4) * 8192 + voff;
      float y[16];
#pragma unroll
      for (int t = 0; t < 4; ++t) { const u32x4 kk = *(const u32x4*)(pk + t * 1024); qk4(kk, q, y + 4 * t); }
      const float p2 = softmax_step(y, m, l, acc, b0s, b1s, b2s, b3s);
      { const u32x4 vv = *(const u32x4*)(pk + 4096); pv_step<0>(vv, p2, acc); }
      { const u32x4 vv = *(const u32x4*)(pk + 4096 + 1024); pv_step<1>(vv, p2, acc); }
      { const u32x4 vv = *(const u32x4*)(pk + 4096 + 2048); pv_step<2>(vv, p2, acc); }
      { const u32x4 vv = *(const u32x4*)(pk + 4096 + 3072); pv_step<3>(vv, p2, acc); }
      PIN8(acc, 0); PIN8(acc, 8); PIN8(acc, 16); PIN8(acc, 24);
      __builtin_amdgcn_sched_barrier(0);
    }
  }
  STAMP(7);

  {
    l += dpp<ROR4>(l); l += dpp<XOR8>(l);
    const float M = swmax32(swmax16(m));
    const float f = __builtin_amdgcn_exp2f(m - M);
    l *= f;
    l = swadd16(l, l); l = swadd32(l, l);
    const float a0 = dpp<0x00>(f), a1 = dpp<0x55>(f), a2 = dpp<0xAA>(f), a3 = dpp<0xFF>(f);
#pragma unroll
    for (int k = 0; k < 8; ++k) { acc[k] *= a0; acc[8 + k] *= a1; acc[16 + k] *= a2; acc[24 + k] *= a3; }
#pragma unroll
    for (int k = 0; k < 16; ++k) acc[k] = swadd32(acc[k], acc[k + 16]);
#pragma unroll
    for (int k = 0; k < 8; ++k) acc[k] = swadd16(acc[k], acc[k + 8]);
    float* rec = (float*)(lds + L_MRG) + w * 512 + r * 128 + 8 * c;
    *(f32x4*)rec = f32x4{acc[0], acc[1], acc[2], acc[3]};
    *(f32x4*)(rec + 4) = f32x4{acc[4], acc[5], acc[6], acc[7]};
    if (lane < 4) { ((float*)(lds + L_MM))[lane * 16 + w] = M; ((float*)(lds + L_LL))[lane * 16 + w] = l; }
  }
  LDS_BARRIER();
  STAMP(8);
  const int ncol = onc * 64 + 8 * (lane & 7) + (lane >> 3);
  if (w < 8) {
    const int g = w >> 1, d = tid & 127;
    f32x4 mv[4], lv[4];
#pragma unroll
    for (int i = 0; i < 4; ++i) { mv[i] = *(const f32x4*)(lds + L_MM + g * 64 + i * 16); lv[i] = *(const f32x4*)(lds + L_LL + g * 64 + i * 16); }
    float M = -INFINITY;
#pragma unroll
    for (int i = 0; i < 16; ++i) M = fmaxf(M, mv[i >> 2][i & 3]);
    M = __uint_as_float(__float_as_uint(M) & 0xffffff00u);
    float L = 0.f, o = 0.f;
#pragma unroll
    for (int i = 0; i < 16; ++i) {
      const float wt = __builtin_amdgcn_exp2f(mv[i >> 2][i & 3] - M);
      L = fmaf(wt, lv[i >> 2][i & 3], L);
      o = fmaf(wt, ((const float*)(lds + L_MRG))[i * 512 + tid], o);
    }
    const int rec = (ab * HQ + akv * 4 + g) * 8 + xg;
    __builtin_amdgcn_raw_buffer_store_b64(gpack(o, tlo, thi), rws, WS_PG + (rec * 128 + d) * 8, 0, AUX_SC1);
    u32x2 ml = gpack(d == 0 ? M : L, tlo, thi);
    if (d == 0) ml[0] = (__float_as_uint(M) >> 8) | tlo;
    __builtin_amdgcn_raw_buffer_store_b64(ml, rws, d < 2 ? WS_ML + (rec * 2 + d) * 8 : OOB, 0, AUX_SC1);
    STAMP(9);

    const int tb = w >> 1, kh = w & 1, sp = lane >> 3, dd = lane & 7;
    const int crec = (tb * HQ + oh) * 8 + sp;
    const int po = WS_PG + (crec * 128 + kh * 64) * 8 + dd * 16, mo = WS_ML + crec * 16;
    u32x4 pg[4], mg;
    mg = __builtin_amdgcn_raw_buffer_load_b128(rws, mo, 0, AUX_SC1);
#pragma unroll
    for (int i = 0; i < 4; ++i) pg[i] = __builtin_amdgcn_raw_buffer_load_b128(rws, po + i * 128, 0, AUX_SC1);
    __builtin_amdgcn_sched_barrier(0);
    u32x4 wc[8];
#pragma unroll
    for (int i = 0; i < 8; ++i) wc[i] = *(const u32x4*)(lds + L_WOT + lane * 272 + kh * 128 + i * 16);
    for (unsigned spins = 0;; ++spins) {
      unsigned bad = gbad(mg[0], mg[1], tlo, thi) | gbad(mg[2], mg[3], tlo, thi);
#pragma unroll
      for (int i = 0; i < 4; ++i) bad |= gbad(pg[i][0], pg[i][1], tlo, thi) | gbad(pg[i][2], pg[i][3], tlo, thi);
      if (__builtin_amdgcn_ballot_w64(bad != 0) == 0ull || spins > SPIN_CAP) break;
      __builtin_amdgcn_s_sleep(4);
      asm volatile("" ::: "memory");
      if (bad) {
        mg = __builtin_amdgcn_raw_buffer_load_b128(rws, mo, 0, AUX_SC1);
#pragma unroll
        for (int i = 0; i < 4; ++i) pg[i] = __builtin_amdgcn_raw_buffer_load_b128(rws, po + i * 128, 0, AUX_SC1);
      }
    }
    STAMP(10);
    const float ms = gval(mg[0]), ls = gval(mg[2]);
    float Mx = fmaxf(ms, dpp<XOR8>(ms));
    Mx = swmax32(swmax16(Mx));
    const float wt = __builtin_amdgcn_exp2f(ms - Mx);
    float Ls = wt * ls;
    Ls += dpp<XOR8>(Ls); Ls = swadd16(Ls, Ls); Ls = swadd32(Ls, Ls);
    float v[8];
#pragma unroll
    for (int i = 0; i < 4; ++i) { v[2 * i] = wt * gval(pg[i][0]); v[2 * i + 1] = wt * gval(pg[i][2]); }
#pragma unroll
    for (int i = 0; i < 4; ++i) v[i] = swadd32(v[i], v[i + 4]);
#pragma unroll
    for (int i = 0; i < 2; ++i) v[i] = swadd16(v[i], v[i + 2]);
    v[0] = trsel<XOR8>(v[0], v[1], b3s);
    const int dim = 2 * dd + ((lane >> 3) & 1) + 16 * (lane >> 4);
    u16* ob = (u16*)(lds + L_O + w * 128);
    ob[dim] = (u16)bfbits(v[0] * __builtin_amdgcn_rcpf(Ls));
    asm volatile("s_waitcnt lgkmcnt(0)" ::: "memory");
    float s = 0.f;
#pragma unroll
    for (int i = 0; i < 8; ++i) {
      const u32x4 ov = *(const u32x4*)(lds + L_O + w * 128 + i * 16);
#pragma unroll
      for (int k = 0; k < 4; ++k) s = dot2(wc[i][k], ov[k], s);
    }
    __builtin_amdgcn_raw_buffer_store_b64(gpack(s, tlo, thi), rws, WS_SG + (((oh * 2 + kh) * NB + tb) * DM + ncol) * 8, 0, AUX_SC1);
    STAMP(11);
  } else if (w < 12) {
    const int tb = w - 8;
    const int ho = lane < 56 ? WS_HG + (tb * FF + oh * 56 + lane) * 8 : WS_HG + (tb * FF + oh * 56) * 8;
    u32x2 hg = __builtin_amdgcn_raw_buffer_load_b64(rws, ho, 0, AUX_SC1);
    u32x4 wc[7];
#pragma unroll
    for (int i = 0; i < 7; ++i) wc[i] = *(const u32x4*)(lds + L_W2T + lane * 144 + i * 16);
    for (unsigned spins = 0;; ++spins) {
      const unsigned bad = gbad(hg[0], hg[1], tlo, thi);
      if (__builtin_amdgcn_ballot_w64(bad != 0) == 0ull || spins > SPIN_CAP) break;
      __builtin_amdgcn_s_sleep(4);
      asm volatile("" ::: "memory");
      hg = __builtin_amdgcn_raw_buffer_load_b64(rws, ho, 0, AUX_SC1);
    }
    u16* ob = (u16*)(lds + L_O + w * 128);
    ob[lane] = (u16)((hg[0] >> 8) & 0xffffu);
    asm volatile("s_waitcnt lgkmcnt(0)" ::: "memory");
    float s = 0.f;
#pragma unroll
    for (int i = 0; i < 7; ++i) {
      const u32x4 ov = *(const u32x4*)(lds + L_O + w * 128 + i * 16);
#pragma unroll
      for (int k = 0; k < 4; ++k) s = dot2(wc[i][k], ov[k], s);
    }
    __builtin_amdgcn_raw_buffer_store_b64(gpack(s, tlo, thi), rws, WS_MG + ((oh * NB + tb) * DM + ncol) * 8, 0, AUX_SC1);
  } else if (w == 12) {
    const int fb = bid >> 6, n0 = (bid & 63) * 8, kq = lane >> 2, p2 = lane & 3;
    const int so = WS_SG + ((kq * NB + fb) * DM + n0 + 2 * p2) * 8, mo = WS_MG + ((kq * NB + fb) * DM + n0 + 2 * p2) * 8;
    constexpr int KSTR = 16 * NB * DM * 8;
    const __amdgpu_buffer_rsrc_t rb = __builtin_amdgcn_make_buffer_rsrc((void*)p.b2, 0, DM * 2, RSRC_W3);
    const unsigned b2v = __builtin_amdgcn_raw_buffer_load_b32(rb, (n0 + 2 * p2) * 2, 0, 0);
    const unsigned xv = __builtin_amdgcn_raw_buffer_load_b32(rx, (fb * DM + n0 + 2 * p2) * 2, 0, 0);
    u32x4 sg[4], mg[2];
    for (unsigned spins = 0;; ++spins) {
#pragma unroll
      for (int i = 0; i < 2; ++i) mg[i] = __builtin_amdgcn_raw_buffer_load_b128(rws, mo + i * KSTR, 0, AUX_SC1);
#pragma unroll
      for (int i = 0; i < 4; ++i) sg[i] = __builtin_amdgcn_raw_buffer_load_b128(rws, so + i * KSTR, 0, AUX_SC1);
      unsigned bad = 0;
#pragma unroll
      for (int i = 0; i < 2; ++i) bad |= gbad(mg[i][0], mg[i][1], tlo, thi) | gbad(mg[i][2], mg[i][3], tlo, thi);
#pragma unroll
      for (int i = 0; i < 4; ++i) bad |= gbad(sg[i][0], sg[i][1], tlo, thi) | gbad(sg[i][2], sg[i][3], tlo, thi);
      if (__builtin_amdgcn_ballot_w64(bad != 0) == 0ull || spins > SPIN_CAP) break;
      __builtin_amdgcn_s_sleep(4);
      asm volatile("" ::: "memory");
    }
    STAMP(12);
    float a[4];
    a[0] = (gval(sg[0][0]) + gval(sg[1][0])) + (gval(sg[2][0]) + gval(sg[3][0]));
    a[1] = (gval(sg[0][2]) + gval(sg[1][2])) + (gval(sg[2][2]) + gval(sg[3][2]));
    a[2] = gval(mg[0][0]) + gval(mg[1][0]);
    a[3] = gval(mg[0][2]) + gval(mg[1][2]);
#pragma unroll
    for (int i = 0; i < 4; ++i) {
      float t = a[i];
      t += dpp<XOR8>(t); t += dpp<ROR4>(t);
      t = swadd16(t, t); t = swadd32(t, t);
      a[i] = t;
    }
    float o[2];
#pragma unroll
    for (int j = 0; j < 2; ++j) {
      const float s = bfr(a[j]);
      const float mm = bfr(bfr(a[2 + j]) + (j ? bfhi(b2v) : bflo(b2v)));
      const float g = bfr(mm * __builtin_amdgcn_rcpf(1.0f + __expf(-mm)));
      o[j] = (j ? bfhi(xv) : bflo(xv)) + bfr(s * g);
    }
    if (lane < 4) *(unsigned*)(p.out + fb * DM + n0 + 2 * p2) = bfbits(o[0]) | (bfbits(o[1]) << 16);
    if (bid == 0 && lane == 0) __hip_atomic_store((GAS u64*)(p.ws + WS_EPOCH), epoch + 1ull, RLX_AGENT);
    STAMP(15);
  }
  asm volatile("" :: "v"(kr[0]), "v"(kr[1]), "v"(kr[2]), "v"(kr[3]), "v"(vr[0]), "v"(vr[1]), "v"(vr[2]), "v"(vr[3]));
}

extern "C" void kernel_launch(void* const* d_in, const int* in_sizes, int n_in,
                              void* d_out, int out_size, void* d_ws, size_t ws_size,
                              hipStream_t stream) {
  Params p;
  p.x = (const u16*)d_in[0]; p.kc = (const u16*)d_in[1]; p.vc = (const u16*)d_in[2];
  p.wq = (const u16*)d_in[3]; p.wo = (const u16*)d_in[4];
  p.w1 = (const u16*)d_in[6]; p.b1 = (const u16*)d_in[7]; p.w2 = (const u16*)d_in[8]; p.b2 = (const u16*)d_in[9];
  p.out = (u16*)d_out; p.ws = (unsigned char*)d_ws;
  hipLaunchKernelGGL(fused_decode_kernel, dim3(NWG), dim3(NT), 0, stream, p);
}
```

```cpp
#include <hip/hip_runtime.h>
#include <hip/hip_bf16.h>
#include <stdint.h>

#define WINDOW 7
#define BAL 1
#define PSLEEP 2
#define NGATH 3
#define GSTAG 10
#define WO_AUX 2
#define MAPX 0
#define BLK 0

typedef unsigned u32x4 __attribute__((ext_vector_type(4)));
typedef unsigned u32x2 __attribute__((ext_vector_type(2)));
typedef float f32x4 __attribute__((ext_vector_type(4)));
typedef __bf16 bf16x2 __attribute__((ext_vector_type(2)));
typedef unsigned short u16;
typedef unsigned long long u64;
#define GAS __attribute__((address_space(1)))
#define LAS __attribute__((address_space(3)))
#define RLX_AGENT __ATOMIC_RELAXED, __HIP_MEMORY_SCOPE_AGENT

constexpr int NB = 4, DM = 512, HQ = 32, HKV = 8, SK = 16384, DH = 128, FF = 1792;
constexpr int NWG = 256, NW = 16, NT = NW * 64;
constexpr int PAGES = SK / 16;
constexpr int AUX_NT = 2, AUX_SC1 = 16;
constexpr int RSRC_W3 = 0x00020000;
constexpr int WS_EPOCH = 0;
constexpr int WS_QG = 4096;
constexpr int WS_HG = WS_QG + 4 * NB * 4096 * 8;
constexpr int WS_ML = WS_HG + 8 * NB * FF * 8;
constexpr int WS_PG = WS_ML + NB * HQ * 8 * 2 * 8;
constexpr int WS_SG = WS_PG + NB * HQ * 8 * 128 * 8;
constexpr int WS_MG = WS_SG + 64 * NB * DM * 8;
constexpr int WS_END = WS_MG + 32 * NB * DM * 8;
constexpr int WS_STAMP = 8 << 20;
constexpr int OOB = 0x7ff00000;
constexpr int L_CM = 0;
constexpr int L_PARK = L_CM + 12 * 2048;
constexpr int L_W2T = L_PARK + 12 * 8192;
constexpr int L_MM = L_W2T + 64 * 144;
constexpr int L_LL = L_MM + 256;
constexpr int L_RQ = L_LL + 256;
constexpr int L_RH = L_RQ + 4096;
constexpr int L_Q = L_RH + 4096;
constexpr int L_O = L_Q + 1024;
constexpr int L_XN = L_O + 5 * 128;
constexpr int L_Y = L_XN + 4096;
constexpr int L_TAG = L_Y + 4096;
constexpr int L_END = L_TAG + 48;
constexpr unsigned SPIN_CAP = 1u << 16;

struct Params {
  const u16* x; const u16* kc; const u16* vc; const u16* wq; const u16* wo;
  const u16* w1; const u16* b1; const u16* w2; const u16* b2;
  u16* out; unsigned char* ws;
};

__device__ __forceinline__ float dot2(unsigned a, unsigned b, float acc) {
  return __builtin_amdgcn_fdot2_f32_bf16(__builtin_bit_cast(bf16x2, a), __builtin_bit_cast(bf16x2, b), acc, false);
}
template <int CTRL> __device__ __forceinline__ float dpp(float x) {
  return __builtin_bit_cast(float, __builtin_amdgcn_mov_dpp(__builtin_bit_cast(int, x), CTRL, 0xf, 0xf, true));
}
constexpr int XOR1 = 0xB1, XOR2 = 0x4E, XOR7 = 0x141, XOR8 = 0x128, ROR4 = 0x124, NEWBC = 0x150;
__device__ __forceinline__ float bfr(float x) { return (float)(__bf16)x; }
__device__ __forceinline__ unsigned bfbits(float x) { return (unsigned)__builtin_bit_cast(u16, (__bf16)x); }
__device__ __forceinline__ float bflo(unsigned u) { return __builtin_bit_cast(float, u << 16); }
__device__ __forceinline__ float bfhi(unsigned u) { return __builtin_bit_cast(float, u & 0xffff0000u); }
__device__ __forceinline__ float swadd32(float a, float b) {
  auto s = __builtin_amdgcn_permlane32_swap(__float_as_uint(a), __float_as_uint(b), false, false);
  return __uint_as_float(s[0]) + __uint_as_float(s[1]);
}
__device__ __forceinline__ float swadd16(float a, float b) {
  auto s = __builtin_amdgcn_permlane16_swap(__float_as_uint(a), __float_as_uint(b), false, false);
  return __uint_as_float(s[0]) + __uint_as_float(s[1]);
}
__device__ __forceinline__ float swmax32(float a) {
  auto s = __builtin_amdgcn_permlane32_swap(__float_as_uint(a), __float_as_uint(a), false, false);
  return fmaxf(__uint_as_float(s[0]), __uint_as_float(s[1]));
}
__device__ __forceinline__ float swmax16(float a) {
  auto s = __builtin_amdgcn_permlane16_swap(__float_as_uint(a), __float_as_uint(a), false, false);
  return fmaxf(__uint_as_float(s[0]), __uint_as_float(s[1]));
}
__device__ __forceinline__ float wave_sum(float v) {
  v += dpp<XOR1>(v); v += dpp<XOR2>(v); v += dpp<XOR7>(v); v += dpp<XOR8>(v);
  v = swadd16(v, v); v = swadd32(v, v);
  return v;
}
template <int CTRL> __device__ __forceinline__ float trsel(float lo, float hi, bool sel) {
  const float t1 = lo + dpp<CTRL>(lo), t2 = hi + dpp<CTRL>(hi);
  return sel ? t2 : t1;
}
__device__ __forceinline__ float divr(float a, float d, float r) {
  const float q = a * r;
  return fmaf(fmaf(-q, d, a), r, q);
}
__device__ __forceinline__ u32x2 gpack(float v, unsigned tlo, unsigned thi) {
  const unsigned b = __float_as_uint(v) + 0x80u;
  return u32x2{(b >> 8) | tlo, thi};
}
__device__ __forceinline__ unsigned gbad(unsigned lo, unsigned hi, unsigned tlo, unsigned thi) {
  return (hi ^ thi) | ((lo ^ tlo) & 0xff000000u);
}
__device__ __forceinline__ float gval(unsigned lo) { return __uint_as_float(lo << 8); }
#define LDS_BARRIER() do { asm volatile("s_waitcnt lgkmcnt(0)" ::: "memory"); __builtin_amdgcn_s_barrier(); asm volatile("" ::: "memory"); } while (0)

#define STAMP(i) do {} while (0)

__device__ __forceinline__ void layernorm8(float (&v)[8]) {
  const float s = ((v[0] + v[1]) + (v[2] + v[3])) + ((v[4] + v[5]) + (v[6] + v[7]));
  const float mean = wave_sum(s) * (1.0f / DM);
  float q0 = 0.f, q1 = 0.f;
#pragma unroll
  for (int i = 0; i < 8; i += 2) { const float d0 = v[i] - mean, d1 = v[i + 1] - mean; q0 = fmaf(d0, d0, q0); q1 = fmaf(d1, d1, q1); }
  const float var = wave_sum(q0 + q1) * (1.0f / DM);
  const float mb = bfr(mean);
  const float den = bfr(__builtin_amdgcn_sqrtf(bfr(bfr(var) + 1e-5f)));
  const float rc = __builtin_amdgcn_rcpf(den);
#pragma unroll
  for (int i = 0; i < 8; ++i) v[i] = bfr(divr(bfr(v[i] - mb), den, rc));
}
__device__ __forceinline__ u32x4 pack8(const float (&v)[8]) {
  u32x4 o;
#pragma unroll
  for (int i = 0; i < 4; ++i) o[i] = (__float_as_uint(v[2 * i]) >> 16) | (__float_as_uint(v[2 * i + 1]) & 0xffff0000u);
  return o;
}
#define PIN8(a, k) asm volatile("" : "+v"(a[k]), "+v"(a[k + 1]), "+v"(a[k + 2]), "+v"(a[k + 3]), "+v"(a[k + 4]), "+v"(a[k + 5]), "+v"(a[k + 6]), "+v"(a[k + 7]))

__device__ __forceinline__ float softmax_step(float (&x)[16], float& m, float& l, float (&acc)[32], bool b0s, bool b1s, bool b2s, bool b3s) {
  constexpr float SCALE = 0.088388347648318440f, L2E = 1.4426950408889634f;
#pragma unroll
  for (int h = 0; h < 8; ++h) x[h] = trsel<XOR8>(x[h], x[h + 8], b3s);
#pragma unroll
  for (int h = 0; h < 4; ++h) x[h] = trsel<XOR7>(x[h], x[h + 4], b2s);
#pragma unroll
  for (int h = 0; h < 2; ++h) x[h] = trsel<XOR2>(x[h], x[h + 2], b1s);
  x[0] = trsel<XOR1>(x[0], x[1], b0s);
  const float sc = bfr(bfr(x[0]) * SCALE) * L2E;
  float pm = fmaxf(sc, dpp<ROR4>(sc));
  pm = fmaxf(pm, dpp<XOR8>(pm));
  const float mn = fmaxf(m, pm);
  const float alpha = __builtin_amdgcn_exp2f(m - mn), pr = __builtin_amdgcn_exp2f(sc - mn);
  l = fmaf(l, alpha, pr); m = mn;
  const float a0 = dpp<0x00>(alpha), a1 = dpp<0x55>(alpha), a2 = dpp<0xAA>(alpha), a3 = dpp<0xFF>(alpha);
#pragma unroll
  for (int k = 0; k < 8; ++k) { acc[k] *= a0; acc[8 + k] *= a1; acc[16 + k] *= a2; acc[24 + k] *= a3; }
  return pr;
}
template <int T> __device__ __forceinline__ void pv_step(const u32x4& v, float pr, float (&acc)[32]) {
  float vf[8];
#pragma unroll
  for (int k = 0; k < 4; ++k) { vf[2 * k] = bflo(v[k]); vf[2 * k + 1] = bfhi(v[k]); }
  const float p0 = dpp<NEWBC + 4 * T + 0>(pr), p1 = dpp<NEWBC + 4 * T + 1>(pr), p2 = dpp<NEWBC + 4 * T + 2>(pr), p3 = dpp<NEWBC + 4 * T + 3>(pr);
#pragma unroll
  for (int k = 0; k < 8; ++k) {
    acc[k] = fmaf(p0, vf[k], acc[k]); acc[8 + k] = fmaf(p1, vf[k], acc[8 + k]);
    acc[16 + k] = fmaf(p2, vf[k], acc[16 + k]); acc[24 + k] = fmaf(p3, vf[k], acc[24 + k]);
  }
}
__device__ __forceinline__ void qk4(const u32x4& k, const u32x4 (&q)[4], float* x) {
#pragma unroll
  for (int g = 0; g < 4; ++g) {
    float d = 0.f;
#pragma unroll
    for (int i = 0; i < 4; ++i) d = dot2(k[i], q[g][i], d);
    x[g] = d;
  }
}

template <int NP> __device__ __forceinline__ f32x4 proj_tile(const u32x4 (&wt)[2 * NP], const unsigned char* act, int row0, bool b3s) {
  float y[32];
#pragma unroll
  for (int i = 0; i < 32; ++i) y[i] = 0.f;
#pragma unroll
  for (int pp = 0; pp < NP; ++pp) {
    unsigned apk[4];
#pragma unroll
    for (int b = 0; b < 4; ++b) apk[b] = *(const unsigned*)(act + (b * DM + row0 + 16 * pp) * 2);
#pragma unroll
    for (int k = 0; k < 4; ++k) {
      const unsigned lo = __builtin_amdgcn_perm(wt[2 * pp + 1][k], wt[2 * pp][k], 0x05040100u);
      const unsigned hi = __builtin_amdgcn_perm(wt[2 * pp + 1][k], wt[2 * pp][k], 0x07060302u);
#pragma unroll
      for (int b = 0; b < 4; ++b) { y[b * 8 + 2 * k] = dot2(lo, apk[b], y[b * 8 + 2 * k]); y[b * 8 + 2 * k + 1] = dot2(hi, apk[b], y[b * 8 + 2 * k + 1]); }
    }
  }
#pragma unroll
  for (int h = 0; h < 16; ++h) y[h] = swadd32(y[h], y[h + 16]);
#pragma unroll
  for (int h = 0; h < 8; ++h) y[h] = swadd16(y[h], y[h + 8]);
#pragma unroll
  for (int h = 0; h < 4; ++h) y[h] = trsel<XOR8>(y[h], y[h + 4], b3s);
  return f32x4{y[0], y[1], y[2], y[3]};
}
__device__ __forceinline__ void lds_arrive(LAS unsigned* cnt, int lane) {
  asm volatile("s_waitcnt lgkmcnt(0)" ::: "memory");
  if (lane == 0) __hip_atomic_fetch_add(cnt, 1u, __ATOMIC_RELAXED, __HIP_MEMORY_SCOPE_WORKGROUP);
}
template <int SLEEP> __device__ __forceinline__ void lds_wait(LAS unsigned* cnt, unsigned target) {
#pragma nounroll
  for (unsigned spins = 0; spins < (1u << 24); ++spins) {
    const unsigned v = __builtin_amdgcn_readfirstlane(__hip_atomic_load(cnt, __ATOMIC_RELAXED, __HIP_MEMORY_SCOPE_WORKGROUP));
    if (v >= target) break;
    if (SLEEP) __builtin_amdgcn_s_sleep(SLEEP);
  }
  asm volatile("" ::: "memory");
}

__global__ void __launch_bounds__(NT, 4) fused_decode_kernel(Params p) {
  __shared__ __attribute__((aligned(16))) unsigned char lds[L_END];
  const int bid = blockIdx.x, tid = threadIdx.x, lane = tid & 63;
  const int w = __builtin_amdgcn_readfirstlane(tid >> 6);
  const int r = lane >> 4, c = lane & 15;
  const int xg = bid & 7, yg = bid >> 3;
  STAMP(0);
  LAS unsigned* sync_cnt = (LAS unsigned*)(lds + L_TAG + 8);
  LAS unsigned* q_cnt = (LAS unsigned*)(lds + L_TAG + 12);
  LAS unsigned* tk_take = (LAS unsigned*)(lds + L_TAG + 16);
  LAS unsigned* tk_give = (LAS unsigned*)(lds + L_TAG + 20);
  LAS unsigned* o_flag = (LAS unsigned*)(lds + L_TAG + 24);
  LAS unsigned* rem_cnt = (LAS unsigned*)(lds + L_TAG + 36);
#define TICKET() do { unsigned tk_ = 0; \
    if (lane == 0) tk_ = __hip_atomic_fetch_add(tk_take, 1u, __ATOMIC_RELAXED, __HIP_MEMORY_SCOPE_WORKGROUP); \
    tk_ = __builtin_amdgcn_readfirstlane(tk_); \
    lds_wait<1>(tk_give, (unsigned)max(0, (int)tk_ + 1 - WINDOW)); __builtin_amdgcn_sched_barrier(0); } while (0)
#define LANDED() do { asm volatile("" :: "v"(kr[0]), "v"(kr[1]), "v"(kr[2]), "v"(kr[3])); \
    if (lane == 0) __hip_atomic_fetch_add(tk_give, 1u, __ATOMIC_RELAXED, __HIP_MEMORY_SCOPE_WORKGROUP); __builtin_amdgcn_sched_barrier(0); } while (0)
  const __amdgpu_buffer_rsrc_t rws = __builtin_amdgcn_make_buffer_rsrc((void*)p.ws, 0, WS_END, RSRC_W3);
  const __amdgpu_buffer_rsrc_t rx = __builtin_amdgcn_make_buffer_rsrc((void*)p.x, 0, NB * DM * 2, RSRC_W3);
  const int ab = yg >> 3, akv = xg, asp = yg & 7;
  const size_t kvbase = (size_t)(ab * HKV + akv) * SK * DH;
  const __amdgpu_buffer_rsrc_t rk = __builtin_amdgcn_make_buffer_rsrc((void*)(p.kc + kvbase), 0, PAGES * 4096, RSRC_W3);
  const __amdgpu_buffer_rsrc_t rv = __builtin_amdgcn_make_buffer_rsrc((void*)(p.vc + kvbase), 0, PAGES * 4096, RSRC_W3);
  const int voff = lane * 16;
#define PAGE_OF(i_) ((((int)(i_)) >> 4) * 128 + asp * 16 + (((int)(i_)) & 15))
  const int i0 = w < 12 ? w - 4 : 120 + 2 * (w - 12), istep = w < 12 ? 8 : 1;
  const unsigned iend = w < 12 ? 120u : (unsigned)(i0 + 2);
  const bool b0s = lane & 1, b1s = lane & 2, b2s = lane & 4, b3s = lane & 8;
  const int oh = yg, onc = xg;
  const int okc = akv * 8 + asp;
#define LOAD_PAGE(so_) do { \
    _Pragma("unroll") for (int t = 0; t < 4; ++t) kr[t] = __builtin_amdgcn_raw_buffer_load_b128(rk, voff + t * 1024, (so_), AUX_NT); \
    _Pragma("unroll") for (int t = 0; t < 4; ++t) vr[t] = __builtin_amdgcn_raw_buffer_load_b128(rv, voff + t * 1024, (so_), AUX_NT); } while (0)
#define MAKE_TAG(e0_, e1_) do { \
    epoch = ((u64)__builtin_amdgcn_readfirstlane(e1_) << 32) | (u64)__builtin_amdgcn_readfirstlane(e0_); \
    const u64 tg_ = ((epoch + 1ull) * 0x9E3779B97F4A7C15ull) >> 24; tlo = (unsigned)(tg_ & 0xffu) << 24; thi = (unsigned)(tg_ >> 8); } while (0)

  u32x4 kr[4], vr[4];
  unsigned tlo, thi;
  u64 epoch;
  if (w < 4) {
    __builtin_amdgcn_s_setprio(3);
    const int cn = lane & 7, rp = lane >> 3;
    const int qrow = (yg >> 3) * 128 + w * 32 + 2 * rp, qcol = xg * 512 + (yg & 7) * 64;
    const int hrow = xg * 64 + w * 16 + 2 * rp, hcol = yg * 64;
    const bool has_mlp = yg < 28;
    if (tid == 0) { *sync_cnt = 0u; *q_cnt = 0u; *tk_take = 0u; *tk_give = 0u; *o_flag = 0u; *rem_cnt = 0u; }
    const u32x4 xa = __builtin_amdgcn_raw_buffer_load_b128(rx, (w * DM + lane * 8) * 2, 0, 0);
    u32x4 wqv[4], w1v[2];
    {
      const __amdgpu_buffer_rsrc_t rq = __builtin_amdgcn_make_buffer_rsrc((void*)p.wq, 0, DM * 4096 * 2, RSRC_W3);
      const int o = (qrow * 4096 + qcol + cn * 8) * 2;
      wqv[0] = __builtin_amdgcn_raw_buffer_load_b128(rq, o, 0, AUX_NT);
      wqv[1] = __builtin_amdgcn_raw_buffer_load_b128(rq, o + 8192, 0, AUX_NT);
      wqv[2] = __builtin_amdgcn_raw_buffer_load_b128(rq, o + 16 * 8192, 0, AUX_NT);
      wqv[3] = __builtin_amdgcn_raw_buffer_load_b128(rq, o + 17 * 8192, 0, AUX_NT);
    }
    const u32x2 e = __builtin_amdgcn_raw_buffer_load_b64(rws, WS_EPOCH, 0, AUX_SC1);
    {
      const __amdgpu_buffer_rsrc_t r1s = __builtin_amdgcn_make_buffer_rsrc((void*)p.w1, 0, DM * FF * 2, RSRC_W3);
      const int o = has_mlp ? (hrow * FF + hcol + cn * 8) * 2 : OOB;
      w1v[0] = __builtin_amdgcn_raw_buffer_load_b128(r1s, o, 0, AUX_NT);
      w1v[1] = __builtin_amdgcn_raw_buffer_load_b128(r1s, o + FF * 2, 0, AUX_NT);
    }
    __builtin_amdgcn_sched_barrier(0);
    LDS_BARRIER();
    __builtin_amdgcn_sched_barrier(0);
    STAMP(1);
    float lnv[8];
#pragma unroll
    for (int i = 0; i < 4; ++i) { lnv[2 * i] = bflo(xa[i]); lnv[2 * i + 1] = bfhi(xa[i]); }
    layernorm8(lnv);
    *(u32x4*)(lds + L_XN + (w * DM + lane * 8) * 2) = pack8(lnv);
    lds_arrive(sync_cnt, lane);
    lds_wait<0>(sync_cnt, 4);
    STAMP(2);
    {
      const f32x4 y = proj_tile<2>(wqv, lds + L_XN, qrow, b3s);
      *(f32x4*)(lds + L_RQ + ((w * 4 + r) * 64 + cn * 8 + (b3s ? 4 : 0)) * 4) = y;
    }
    MAKE_TAG(e[0], e[1]);
    if (tid == 0) *(u32x2*)(lds + L_TAG) = e;
    lds_arrive(sync_cnt, lane);
    lds_wait<0>(sync_cnt, 8);
    STAMP(3);
    {
      const float* rq = (const float*)(lds + L_RQ) + w * 64 + lane;
      const float sq = (rq[0] + rq[256]) + (rq[512] + rq[768]);
      __builtin_amdgcn_raw_buffer_store_b64(gpack(sq, tlo, thi), rws, WS_QG + (((yg >> 3) * NB + w) * 4096 + qcol + lane) * 8, 0, AUX_SC1);
    }
    STAMP(4);
    layernorm8(lnv);
    *(u32x4*)(lds + L_Y + (w * DM + lane * 8) * 2) = pack8(lnv);
    lds_arrive(sync_cnt, lane);
    lds_wait<0>(sync_cnt, 12);
    {
      const f32x4 y = proj_tile<1>(w1v, lds + L_Y, hrow, b3s);
      *(f32x4*)(lds + L_RH + ((w * 4 + r) * 64 + cn * 8 + (b3s ? 4 : 0)) * 4) = y;
    }
    lds_arrive(sync_cnt, lane);
    u32x4 qg[4];
    const int qgo = WS_QG + (ab * 4096 + akv * 512 + w * 128) * 8 + lane * 16;
    constexpr int QSTR = NB * 4096 * 8;
#pragma unroll
    for (int i = 0; i < 4; ++i) qg[i] = __builtin_amdgcn_raw_buffer_load_b128(rws, qgo + i * QSTR, 0, AUX_SC1);
    __builtin_amdgcn_sched_barrier(0);
    lds_wait<0>(sync_cnt, 16);
    {
      const float* rh = (const float*)(lds + L_RH) + w * 64 + lane;
      const float sh = (rh[0] + rh[256]) + (rh[512] + rh[768]);
      __builtin_amdgcn_raw_buffer_store_b64(gpack(sh, tlo, thi), rws, has_mlp ? WS_HG + ((xg * NB + w) * FF + hcol + lane) * 8 : OOB, 0, AUX_SC1);
    }
    STAMP(5);
    for (unsigned spins = 0;; ++spins) {
      unsigned bad = 0;
#pragma unroll
      for (int i = 0; i < 4; ++i) bad |= gbad(qg[i][0], qg[i][1], tlo, thi) | gbad(qg[i][2], qg[i][3], tlo, thi);
      if (__builtin_amdgcn_ballot_w64(bad != 0) == 0ull || spins > SPIN_CAP) break;
      STAMP(13);
      asm volatile("" ::: "memory");
#pragma unroll
      for (int i = 0; i < 4; ++i) qg[i] = __builtin_amdgcn_raw_buffer_load_b128(rws, qgo + i * QSTR, 0, AUX_SC1);
    }
    {
      const float q0 = (gval(qg[0][0]) + gval(qg[1][0])) + (gval(qg[2][0]) + gval(qg[3][0]));
      const float q1 = (gval(qg[0][2]) + gval(qg[1][2])) + (gval(qg[2][2]) + gval(qg[3][2]));
      ((unsigned*)(lds + L_Q))[w * 64 + lane] = bfbits(q0) | (bfbits(q1) << 16);
    }
    lds_arrive(q_cnt, lane);
    __builtin_amdgcn_s_setprio(0);
#pragma unroll
    for (int t = 0; t < 4; ++t) { kr[t] = u32x4{0u, 0u, 0u, 0u}; vr[t] = u32x4{0u, 0u, 0u, 0u}; }
  } else {
    __builtin_amdgcn_s_barrier();
    asm volatile("" ::: "memory");
    TICKET();
    LOAD_PAGE(PAGE_OF(i0) * 4096);
    const int t2 = tid - 768, kp = t2 >> 3, cn = t2 & 7;
    const __amdgpu_buffer_rsrc_t rw2 = __builtin_amdgcn_make_buffer_rsrc((void*)p.w2, 0, FF * DM * 2, RSRC_W3);
    const int oo = (w >= 12 && kp < 28) ? ((oh * 56 + 2 * kp) * DM + onc * 64 + cn * 8) * 2 : OOB;
    const u32x4 ua = __builtin_amdgcn_raw_buffer_load_b128(rw2, oo, 0, AUX_NT);
    const u32x4 ub = __builtin_amdgcn_raw_buffer_load_b128(rw2, oo + DM * 2, 0, AUX_NT);
    __builtin_amdgcn_sched_barrier(0);
    STAMP(1);
    LANDED();
    unsigned char* pk = lds + L_PARK + (w - 4) * 8192 + voff;
#pragma unroll
    for (int t = 0; t < 4; ++t) *(u32x4*)(pk + t * 1024) = kr[t];
#pragma unroll
    for (int t = 0; t < 4; ++t) *(u32x4*)(pk + 4096 + t * 1024) = vr[t];
    __builtin_amdgcn_sched_barrier(0);
    STAMP(2);
    if (w >= 12) {
      unsigned char* dst = lds + L_W2T + cn * 144 + kp * 4;
#pragma unroll
      for (int k = 0; k < 4; ++k) {
        *(unsigned*)(dst + (2 * k) * 8 * 144) = __builtin_amdgcn_perm(ub[k], ua[k], 0x05040100u);
        *(unsigned*)(dst + (2 * k + 1) * 8 * 144) = __builtin_amdgcn_perm(ub[k], ua[k], 0x07060302u);
      }
    }
    asm volatile("" :: "v"(ua), "v"(ub));
    TICKET();
    LOAD_PAGE(PAGE_OF(i0 + istep) * 4096);
    __builtin_amdgcn_sched_barrier(0);
    STAMP(3);
    LANDED();
  }
  lds_wait<2>(q_cnt, 4);
  {
    const u32x2 ep = *(const u32x2*)(lds + L_TAG);
    MAKE_TAG(ep[0], ep[1]);
  }
  STAMP(6);

  float m = -INFINITY, l = 0.f, acc[32];
#pragma unroll
  for (int k = 0; k < 32; ++k) acc[k] = 0.f;
  if (w >= 4) {
    u32x4 q[4];
#pragma unroll
    for (int g = 0; g < 4; ++g) q[g] = *(const u32x4*)(lds + L_Q + g * 256 + c * 16);
    unsigned nxt = (unsigned)(i0 + 2 * istep);
    unsigned parkat = nxt;
    asm volatile("" : "+v"(parkat));
    parkat = __builtin_amdgcn_readfirstlane(parkat);
    bool more;
    do {
      float x[16];
#pragma unroll
      for (int t = 0; t < 4; ++t) qk4(kr[t], q, x + 4 * t);
      PIN8(x, 0); PIN8(x, 8);
      more = nxt < iend;
      const int so = more ? PAGE_OF(nxt) * 4096 : OOB;
      __builtin_amdgcn_sched_barrier(0);
#pragma unroll
      for (int t = 0; t < 4; ++t) kr[t] = __builtin_amdgcn_raw_buffer_load_b128(rk, voff + t * 1024, so, AUX_NT);
      __builtin_amdgcn_sched_barrier(0);
      const float pr = softmax_step(x, m, l, acc, b0s, b1s, b2s, b3s);
      pv_step<0>(vr[0], pr, acc); pv_step<1>(vr[1], pr, acc); pv_step<2>(vr[2], pr, acc); pv_step<3>(vr[3], pr, acc);
      PIN8(acc, 0); PIN8(acc, 8); PIN8(acc, 16); PIN8(acc, 24);
      __builtin_amdgcn_sched_barrier(0);
#pragma unroll
      for (int t = 0; t < 4; ++t) vr[t] = __builtin_amdgcn_raw_buffer_load_b128(rv, voff + t * 1024, so, AUX_NT);
      __builtin_amdgcn_sched_barrier(0);
      if (istep == 8) {
        unsigned old = 0;
        if (lane == 0) old = __hip_atomic_fetch_add(rem_cnt, 1u, __ATOMIC_RELAXED, __HIP_MEMORY_SCOPE_WORKGROUP);
        old = __builtin_amdgcn_readfirstlane(old);
        if (nxt - 16u > old) __builtin_amdgcn_s_setprio(0); else __builtin_amdgcn_s_setprio(1);
      }
      if (nxt == parkat) {
        const unsigned char* pk = lds + L_PARK + (w - 4) * 8192 + voff;
        float y[16];
#pragma unroll
        for (int t = 0; t < 4; ++t) { const u32x4 kk = *(const u32x4*)(pk + t * 1024); qk4(kk, q, y + 4 * t); }
        const float p2 = softmax_step(y, m, l, acc, b0s, b1s, b2s, b3s);
        { const u32x4 vv = *(const u32x4*)(pk + 4096); pv_step<0>(vv, p2, acc); }
        { const u32x4 vv = *(const u32x4*)(pk + 4096 + 1024); pv_step<1>(vv, p2, acc); }
        { const u32x4 vv = *(const u32x4*)(pk + 4096 + 2048); pv_step<2>(vv, p2, acc); }
        { const u32x4 vv = *(const u32x4*)(pk + 4096 + 3072); pv_step<3>(vv, p2, acc); }
        PIN8(acc, 0); PIN8(acc, 8); PIN8(acc, 16); PIN8(acc, 24);
        __builtin_amdgcn_sched_barrier(0);
      }
      nxt += istep;
    } while (more);
  } else {
    m = -1e30f;
  }
  STAMP(7);
  const bool is_op = w >= 4 && w < 12;
  {
    const __amdgpu_buffer_rsrc_t ro = __builtin_amdgcn_make_buffer_rsrc((void*)p.wo, 0, 4096 * DM * 2, RSRC_W3);
    const int vo = is_op ? (lane >> 3) * 2048 + (lane & 7) * 16 : OOB;
    const int so = okc * 65536 + (w - 4) * 128;
#pragma unroll
    for (int t = 0; t < 4; ++t) {
      kr[t] = __builtin_amdgcn_raw_buffer_load_b128(ro, vo, so + t * 16384, WO_AUX);
      vr[t] = __builtin_amdgcn_raw_buffer_load_b128(ro, vo + 1024, so + t * 16384, WO_AUX);
    }
  }
  __builtin_amdgcn_sched_barrier(0);

  {
    l += dpp<ROR4>(l); l += dpp<XOR8>(l);
    const float M = swmax32(swmax16(m));
    const float f = __builtin_amdgcn_exp2f(m - M);
    l *= f;
    l = swadd16(l, l); l = swadd32(l, l);
    const float a0 = dpp<0x00>(f), a1 = dpp<0x55>(f), a2 = dpp<0xAA>(f), a3 = dpp<0xFF>(f);
#pragma unroll
    for (int k = 0; k < 8; ++k) { acc[k] *= a0; acc[8 + k] *= a1; acc[16 + k] *= a2; acc[24 + k] *= a3; }
#pragma unroll
    for (int k = 0; k < 16; ++k) acc[k] = swadd32(acc[k], acc[k + 16]);
#pragma unroll
    for (int k = 0; k < 8; ++k) acc[k] = swadd16(acc[k], acc[k + 8]);
    float* rec = (float*)(lds + (w < 12 ? L_CM + w * 2048 : L_PARK + (w - 4) * 8192)) + r * 128 + 8 * c;
    *(f32x4*)rec = f32x4{acc[0], acc[1], acc[2], acc[3]};
    *(f32x4*)(rec + 4) = f32x4{acc[4], acc[5], acc[6], acc[7]};
    if (lane < 4) { ((float*)(lds + L_MM))[lane * 16 + w] = M; ((float*)(lds + L_LL))[lane * 16 + w] = l; }
  }
  if (is_op) {
    asm volatile("s_waitcnt lgkmcnt(0)" ::: "memory");
    __builtin_amdgcn_sched_barrier(0);
    __builtin_amdgcn_s_barrier();
    __builtin_amdgcn_sched_barrier(0);
    asm volatile("" : "+v"(kr[0]), "+v"(kr[1]), "+v"(kr[2]), "+v"(kr[3]), "+v"(vr[0]), "+v"(vr[1]), "+v"(vr[2]), "+v"(vr[3]) :: "memory");
    unsigned char* slot = lds + L_PARK + (w - 4) * 8192;
    const int kq = lane >> 3, cn = lane & 7;
#pragma unroll
    for (int t = 0; t < 4; ++t)
#pragma unroll
      for (int j = 0; j < 8; ++j) {
        const int ch = (2 * t + (kq >> 2)) ^ ((4 * j + (cn >> 1)) & 7);
        *(unsigned*)(slot + (j * 8 + cn) * 128 + ch * 16 + (kq & 3) * 4) =
            __builtin_amdgcn_perm(vr[t][j >> 1], kr[t][j >> 1], (j & 1) ? 0x07060302u : 0x05040100u);
      }
    asm volatile("s_waitcnt lgkmcnt(0)" ::: "memory");
    u32x4 wc[8];
#pragma unroll
    for (int i = 0; i < 8; ++i) wc[i] = *(const u32x4*)(slot + lane * 128 + ((i ^ ((lane >> 1) & 7)) << 4));
    const int so = WS_SG + ((okc * NB + ab) * DM + (w - 4) * 64 + 8 * (lane & 7) + (lane >> 3)) * 8;
    asm volatile("" :: "v"(wc[0]), "v"(wc[1]), "v"(wc[2]), "v"(wc[3]), "v"(wc[4]), "v"(wc[5]), "v"(wc[6]), "v"(wc[7]), "v"(so));
    lds_wait<1>(o_flag, 1);
    u32x4 ov[8];
#pragma unroll
    for (int i = 0; i < 8; ++i) ov[i] = *(const u32x4*)(lds + L_O + 512 + i * 16);
    __builtin_amdgcn_sched_barrier(0);
    float s0 = 0.f, s1 = 0.f;
#pragma unroll
    for (int i = 0; i < 8; i += 2)
#pragma unroll
      for (int k = 0; k < 4; ++k) { s0 = dot2(wc[i][k], ov[i][k], s0); s1 = dot2(wc[i + 1][k], ov[i + 1][k], s1); }
    __builtin_amdgcn_raw_buffer_store_b64(gpack(s0 + s1, tlo, thi), rws, so, 0, AUX_SC1);
    STAMP(11);
  } else {
    LDS_BARRIER();
    STAMP(8);
    {
      const int mi = w < 4 ? w : w - 8, g = mi >> 1, e = mi * 64 + lane, d = e & 127;
      f32x4 mv[4], lv[4];
      float rv16[16];
#pragma unroll
      for (int i = 0; i < 4; ++i) mv[i] = *(const f32x4*)(lds + L_MM + g * 64 + i * 16);
#pragma unroll
      for (int i = 0; i < 16; ++i) rv16[i] = *(const float*)(lds + (i < 12 ? L_CM + i * 2048 : L_PARK + (i - 4) * 8192) + e * 4);
#pragma unroll
      for (int i = 0; i < 4; ++i) lv[i] = *(const f32x4*)(lds + L_LL + g * 64 + i * 16);
      __builtin_amdgcn_sched_barrier(0);
      float M = -INFINITY;
#pragma unroll
      for (int i = 0; i < 16; ++i) M = fmaxf(M, mv[i >> 2][i & 3]);
      M = __uint_as_float(__float_as_uint(M) & 0xffffff00u);
      float L = 0.f, o = 0.f;
#pragma unroll
      for (int i = 0; i < 16; ++i) {
        const float wt = __builtin_amdgcn_exp2f(mv[i >> 2][i & 3] - M);
        L = fmaf(wt, lv[i >> 2][i & 3], L);
        o = fmaf(wt, rv16[i], o);
      }
      const int rec = (ab * HQ + akv * 4 + g) * 8 + asp;
      __builtin_amdgcn_raw_buffer_store_b64(gpack(o, tlo, thi), rws, WS_PG + (rec * 128 + d) * 8, 0, AUX_SC1);
      u32x2 ml = gpack(d == 0 ? M : L, tlo, thi);
      if (d == 0) ml[0] = (__float_as_uint(M) >> 8) | tlo;
      __builtin_amdgcn_raw_buffer_store_b64(ml, rws, d < 2 ? WS_ML + (rec * 2 + d) * 8 : OOB, 0, AUX_SC1);
    }
    STAMP(9);
    if (w < 4) {
      const int hu = oh * 56 + (lane < 56 ? lane : 0);
      const int ho = WS_HG + (w * FF + hu) * 8;
      constexpr int HSTR = NB * FF * 8;
      const __amdgpu_buffer_rsrc_t rb = __builtin_amdgcn_make_buffer_rsrc((void*)p.b1, 0, FF * 2, RSRC_W3);
      const unsigned bb = __builtin_amdgcn_raw_buffer_load_b16(rb, hu * 2, 0, 0);
      u32x2 hg[8];
#pragma unroll
      for (int i = 0; i < 8; ++i) hg[i] = __builtin_amdgcn_raw_buffer_load_b64(rws, ho + i * HSTR, 0, AUX_SC1);
      __builtin_amdgcn_sched_barrier(0);
      u32x4 wc[7];
#pragma unroll
      for (int i = 0; i < 7; ++i) wc[i] = *(const u32x4*)(lds + L_W2T + lane * 144 + i * 16);
#pragma nounroll
      for (unsigned spins = 0;; ++spins) {
        unsigned bad = 0;
#pragma unroll
        for (int i = 0; i < 8; ++i) bad |= gbad(hg[i][0], hg[i][1], tlo, thi);
        if (__builtin_amdgcn_ballot_w64(bad != 0) == 0ull || spins >= SPIN_CAP) break;
        __builtin_amdgcn_s_sleep(8);
        asm volatile("" ::: "memory");
#pragma unroll
        for (int i = 0; i < 8; ++i) hg[i] = __builtin_amdgcn_raw_buffer_load_b64(rws, ho + i * HSTR, 0, AUX_SC1);
      }
      const float sh = ((gval(hg[0][0]) + gval(hg[1][0])) + (gval(hg[2][0]) + gval(hg[3][0]))) +
                       ((gval(hg[4][0]) + gval(hg[5][0])) + (gval(hg[6][0]) + gval(hg[7][0])));
      const float u = bfr(bfr(sh) + bflo(bb));
      const float z = 0.7978845608028654f * (u + 0.044715f * u * u * u);
      const float th = 1.0f - 2.0f * __builtin_amdgcn_rcpf(__expf(2.0f * z) + 1.0f);
      u16* ob = (u16*)(lds + L_O + w * 128);
      ob[lane] = (u16)bfbits(0.5f * u * (1.0f + th));
      asm volatile("s_waitcnt lgkmcnt(0)" ::: "memory");
      u32x4 ov[7];
#pragma unroll
      for (int i = 0; i < 7; ++i) ov[i] = *(const u32x4*)(lds + L_O + w * 128 + i * 16);
      __builtin_amdgcn_sched_barrier(0);
      float sm = 0.f;
#pragma unroll
      for (int i = 0; i < 7; ++i)
#pragma unroll
        for (int k = 0; k < 4; ++k) sm = dot2(wc[i][k], ov[i][k], sm);
      __builtin_amdgcn_raw_buffer_store_b64(gpack(sm, tlo, thi), rws,
                                            WS_MG + ((oh * NB + w) * DM + onc * 64 + 8 * (lane & 7) + (lane >> 3)) * 8, 0, AUX_SC1);
    } else if (w < 12 + NGATH) {
      const int sp = lane >> 3, dd = lane & 7;
      const int crec = (ab * HQ + (okc >> 1)) * 8 + sp;
      const int po = WS_PG + (crec * 128 + (okc & 1) * 64) * 8 + dd * 16, mo = WS_ML + crec * 16;
      if (w == 13) __builtin_amdgcn_s_sleep(GSTAG);
      if (w == 14) __builtin_amdgcn_s_sleep(2 * GSTAG);
      u32x4 pg[4], mg;
      unsigned bad = 1u;
      bool done = false;
#pragma nounroll
      for (unsigned spins = 0;; ++spins) {
        if (bad) {
          mg = __builtin_amdgcn_raw_buffer_load_b128(rws, mo, 0, AUX_SC1);
#pragma unroll
          for (int i = 0; i < 4; ++i) pg[i] = __builtin_amdgcn_raw_buffer_load_b128(rws, po + i * 128, 0, AUX_SC1);
        }
        bad = gbad(mg[0], mg[1], tlo, thi) | gbad(mg[2], mg[3], tlo, thi);
#pragma unroll
        for (int i = 0; i < 4; ++i) bad |= gbad(pg[i][0], pg[i][1], tlo, thi) | gbad(pg[i][2], pg[i][3], tlo, thi);
        if (__builtin_amdgcn_ballot_w64(bad != 0) == 0ull || spins >= SPIN_CAP) break;
        if (__builtin_amdgcn_readfirstlane(__hip_atomic_load(o_flag, __ATOMIC_RELAXED, __HIP_MEMORY_SCOPE_WORKGROUP)) != 0u) { done = true; break; }
        __builtin_amdgcn_s_sleep(PSLEEP);
        asm volatile("" ::: "memory");
      }
      if (!done) {
        STAMP(10);
        const float ms = gval(mg[0]), ls = gval(mg[2]);
        float Mx = fmaxf(ms, dpp<XOR8>(ms));
        Mx = swmax32(swmax16(Mx));
        const float wt = __builtin_amdgcn_exp2f(ms - Mx);
        float Ls = wt * ls;
        Ls += dpp<XOR8>(Ls); Ls = swadd16(Ls, Ls); Ls = swadd32(Ls, Ls);
        float v[8];
#pragma unroll
        for (int i = 0; i < 4; ++i) { v[2 * i] = wt * gval(pg[i][0]); v[2 * i + 1] = wt * gval(pg[i][2]); }
#pragma unroll
        for (int i = 0; i < 4; ++i) v[i] = swadd32(v[i], v[i + 4]);
#pragma unroll
        for (int i = 0; i < 2; ++i) v[i] = swadd16(v[i], v[i + 2]);
        v[0] = trsel<XOR8>(v[0], v[1], b3s);
        const int dim = 2 * dd + ((lane >> 3) & 1) + 16 * (lane >> 4);
        ((u16*)(lds + L_O + 512))[dim] = (u16)bfbits(v[0] * __builtin_amdgcn_rcpf(Ls));
        asm volatile("s_waitcnt lgkmcnt(0)" ::: "memory");
        if (lane == 0) __hip_atomic_store(o_flag, 1u, __ATOMIC_RELAXED, __HIP_MEMORY_SCOPE_WORKGROUP);
      }
    } else if (w == 15) {
      const int fb = bid >> 6, n0 = (bid & 63) * 8, kq = lane >> 2, p2 = lane & 3;
      const int so = WS_SG + ((kq * NB + fb) * DM + n0 + 2 * p2) * 8, mo = WS_MG + ((kq * NB + fb) * DM + n0 + 2 * p2) * 8;
      constexpr int KSTR = 16 * NB * DM * 8;
      const __amdgpu_buffer_rsrc_t rb = __builtin_amdgcn_make_buffer_rsrc((void*)p.b2, 0, DM * 2, RSRC_W3);
      const unsigned b2v = __builtin_amdgcn_raw_buffer_load_b32(rb, (n0 + 2 * p2) * 2, 0, 0);
      const unsigned xv = __builtin_amdgcn_raw_buffer_load_b32(rx, (fb * DM + n0 + 2 * p2) * 2, 0, 0);
      u32x4 sg[6];
      unsigned bad[6];
#pragma unroll
      for (int i = 0; i < 6; ++i) { sg[i] = u32x4{0u, 0u, 0u, 0u}; bad[i] = 1u; }
      lds_wait<2>(o_flag, 1);
#pragma nounroll
      for (unsigned spins = 0;; ++spins) {
        u32x4 t[6];
#pragma unroll
        for (int i = 0; i < 6; ++i) {
          const int o = i < 4 ? so + i * KSTR : mo + (i - 4) * KSTR;
          t[i] = __builtin_amdgcn_raw_buffer_load_b128(rws, bad[i] ? o : OOB, 0, AUX_SC1);
        }
        __builtin_amdgcn_sched_barrier(0);
#pragma unroll
        for (int i = 0; i < 6; ++i)
#pragma unroll
          for (int k = 0; k < 4; ++k) sg[i][k] = bad[i] ? t[i][k] : sg[i][k];
        unsigned any = 0;
#pragma unroll
        for (int i = 0; i < 6; ++i) { bad[i] = gbad(sg[i][0], sg[i][1], tlo, thi) | gbad(sg[i][2], sg[i][3], tlo, thi); any |= bad[i]; }
        if (__builtin_amdgcn_ballot_w64(any != 0) == 0ull || spins >= SPIN_CAP) break;
        __builtin_amdgcn_s_sleep(PSLEEP);
        asm volatile("" ::: "memory");
      }
      STAMP(12);
      float a[4];
      a[0] = (gval(sg[0][0]) + gval(sg[1][0])) + (gval(sg[2][0]) + gval(sg[3][0]));
      a[1] = (gval(sg[0][2]) + gval(sg[1][2])) + (gval(sg[2][2]) + gval(sg[3][2]));
      a[2] = gval(sg[4][0]) + gval(sg[5][0]);
      a[3] = gval(sg[4][2]) + gval(sg[5][2]);
#pragma unroll
      for (int i = 0; i < 4; ++i) {
        float t = a[i];
        t += dpp<XOR8>(t); t += dpp<ROR4>(t);
        t = swadd16(t, t); t = swadd32(t, t);
        a[i] = t;
      }
      float o[2];
#pragma unroll
      for (int j = 0; j < 2; ++j) {
        const float sv = bfr(a[j]);
        const float mm = bfr(bfr(a[2 + j]) + (j ? bfhi(b2v) : bflo(b2v)));
        const float g = bfr(mm * __builtin_amdgcn_rcpf(1.0f + __expf(-mm)));
        o[j] = (j ? bfhi(xv) : bflo(xv)) + bfr(sv * g);
      }
      if (lane < 4) *(unsigned*)(p.out + fb * DM + n0 + 2 * p2) = bfbits(o[0]) | (bfbits(o[1]) << 16);
      if (bid == 0 && lane == 0) __hip_atomic_store((GAS u64*)(p.ws + WS_EPOCH), epoch + 1ull, RLX_AGENT);
      STAMP(15);
    }
  }
}

extern "C" void kernel_launch(void* const* d_in, const int* in_sizes, int n_in,
                              void* d_out, int out_size, void* d_ws, size_t ws_size,
                              hipStream_t stream) {
  Params p;
  p.x = (const u16*)d_in[0]; p.kc = (const u16*)d_in[1]; p.vc = (const u16*)d_in[2];
  p.wq = (const u16*)d_in[3]; p.wo = (const u16*)d_in[4];
  p.w1 = (const u16*)d_in[6]; p.b1 = (const u16*)d_in[7]; p.w2 = (const u16*)d_in[8]; p.b2 = (const u16*)d_in[9];
  p.out = (u16*)d_out; p.ws = (unsigned char*)d_ws;
  hipLaunchKernelGGL(fused_decode_kernel, dim3(NWG), dim3(NT), 0, stream, p);
}
```

```cpp
#include <hip/hip_runtime.h>
#include <hip/hip_bf16.h>
#include <stdint.h>

#define WINDOW 7
#define BAL 1
#define ADAPT 0
#define AD1 40
#define AD2 100
#define ADS1 16
#define ADS2 48
#define OUT_AUX 16
#define P12 0
#define W0 6
#define PK 1
#define ROT 32
#define RA 3
#define RB 2
#define VROT 0
#define VOFF(x_) (((x_) + VROT * 65536) & 0x3fffff)
#define PSLEEP 2
#define NGATH 3
#define GSTAG 10
#define WO_AUX 2
#define MAPX 0
#define BLK 2

typedef unsigned u32x4 __attribute__((ext_vector_type(4)));
typedef unsigned u32x2 __attribute__((ext_vector_type(2)));
typedef float f32x4 __attribute__((ext_vector_type(4)));
typedef float f32x2 __attribute__((ext_vector_type(2)));
typedef __bf16 bf16x2 __attribute__((ext_vector_type(2)));
typedef unsigned short u16;
typedef unsigned long long u64;
#define GAS __attribute__((address_space(1)))
#define LAS __attribute__((address_space(3)))
#define RLX_AGENT __ATOMIC_RELAXED, __HIP_MEMORY_SCOPE_AGENT

constexpr int NB = 4, DM = 512, HQ = 32, HKV = 8, SK = 16384, DH = 128, FF = 1792;
constexpr int NWG = 256, NW = 16, NT = NW * 64;
constexpr int PAGES = SK / 16;
#define KV_AUX 2
constexpr int AUX_NT = KV_AUX, AUX_SC1 = 16;
constexpr int RSRC_W3 = 0x00020000;
constexpr int WS_EPOCH = 0;
constexpr int WS_QG = 4096;
constexpr int WS_HG = WS_QG + 4 * NB * 4096 * 8;
constexpr int WS_ML = WS_HG + 8 * NB * FF * 8;
constexpr int WS_PG = WS_ML + NB * HQ * 8 * 2 * 8;
constexpr int WS_SG = WS_PG + NB * HQ * 8 * 128 * 8;
constexpr int WS_MG = WS_SG + 64 * NB * DM * 8;
constexpr int WS_END = WS_MG + 32 * NB * DM * 8;
constexpr int WS_STAMP = 8 << 20;
constexpr int OOB = 0x7ff00000;
constexpr int L_CM = 0;
constexpr int L_PARK = L_CM + 12 * 2048;
constexpr int L_W2T = L_PARK + 12 * 8192;
constexpr int L_MM = L_W2T + 64 * 144;
constexpr int L_LL = L_MM + 256;
constexpr int L_RQ = L_LL + 256;
constexpr int L_RH = L_RQ + 4096;
constexpr int L_Q = L_RH + 4096;
constexpr int L_O = L_Q + 1024;
constexpr int L_XN = L_O + 5 * 128;
constexpr int L_Y = L_XN + 4096;
constexpr int L_TAG = L_Y + 4096;
constexpr int L_END = L_TAG + 48;
constexpr unsigned SPIN_CAP = 1u << 16;

struct Params {
  const u16* x; const u16* kc; const u16* vc; const u16* wq; const u16* wo;
  const u16* w1; const u16* b1; const u16* w2; const u16* b2;
  u16* out; unsigned char* ws;
};

__device__ __forceinline__ float dot2(unsigned a, unsigned b, float acc) {
  return __builtin_amdgcn_fdot2_f32_bf16(__builtin_bit_cast(bf16x2, a), __builtin_bit_cast(bf16x2, b), acc, false);
}
template <int CTRL> __device__ __forceinline__ float dpp(float x) {
  return __builtin_bit_cast(float, __builtin_amdgcn_mov_dpp(__builtin_bit_cast(int, x), CTRL, 0xf, 0xf, true));
}
constexpr int XOR1 = 0xB1, XOR2 = 0x4E, XOR7 = 0x141, XOR8 = 0x128, ROR4 = 0x124, NEWBC = 0x150;
__device__ __forceinline__ float bfr(float x) { return (float)(__bf16)x; }
__device__ __forceinline__ unsigned bfbits(float x) { return (unsigned)__builtin_bit_cast(u16, (__bf16)x); }
__device__ __forceinline__ float bflo(unsigned u) { return __builtin_bit_cast(float, u << 16); }
__device__ __forceinline__ float bfhi(unsigned u) { return __builtin_bit_cast(float, u & 0xffff0000u); }
__device__ __forceinline__ float swadd32(float a, float b) {
  auto s = __builtin_amdgcn_permlane32_swap(__float_as_uint(a), __float_as_uint(b), false, false);
  return __uint_as_float(s[0]) + __uint_as_float(s[1]);
}
__device__ __forceinline__ float swadd16(float a, float b) {
  auto s = __builtin_amdgcn_permlane16_swap(__float_as_uint(a), __float_as_uint(b), false, false);
  return __uint_as_float(s[0]) + __uint_as_float(s[1]);
}
__device__ __forceinline__ float swmax32(float a) {
  auto s = __builtin_amdgcn_permlane32_swap(__float_as_uint(a), __float_as_uint(a), false, false);
  return fmaxf(__uint_as_float(s[0]), __uint_as_float(s[1]));
}
__device__ __forceinline__ float swmax16(float a) {
  auto s = __builtin_amdgcn_permlane16_swap(__float_as_uint(a), __float_as_uint(a), false, false);
  return fmaxf(__uint_as_float(s[0]), __uint_as_float(s[1]));
}
__device__ __forceinline__ float wave_sum(float v) {
  v += dpp<XOR1>(v); v += dpp<XOR2>(v); v += dpp<XOR7>(v); v += dpp<XOR8>(v);
  v = swadd16(v, v); v = swadd32(v, v);
  return v;
}
template <int CTRL> __device__ __forceinline__ float trsel(float lo, float hi, bool sel) {
  const float t1 = lo + dpp<CTRL>(lo), t2 = hi + dpp<CTRL>(hi);
  return sel ? t2 : t1;
}
__device__ __forceinline__ float divr(float a, float d, float r) {
  const float q = a * r;
  return fmaf(fmaf(-q, d, a), r, q);
}
__device__ __forceinline__ u32x2 gpack(float v, unsigned tlo, unsigned thi) {
  const unsigned b = __float_as_uint(v) + 0x80u;
  return u32x2{(b >> 8) | tlo, thi};
}
__device__ __forceinline__ unsigned gbad(unsigned lo, unsigned hi, unsigned tlo, unsigned thi) {
  return (hi ^ thi) | ((lo ^ tlo) & 0xff000000u);
}
__device__ __forceinline__ float gval(unsigned lo) { return __uint_as_float(lo << 8); }
#define LDS_BARRIER() do { asm volatile("s_waitcnt lgkmcnt(0)" ::: "memory"); __builtin_amdgcn_s_barrier(); asm volatile("" ::: "memory"); } while (0)

#define STAMP(i) do {} while (0)

__device__ __forceinline__ void layernorm8(float (&v)[8]) {
  const float s = ((v[0] + v[1]) + (v[2] + v[3])) + ((v[4] + v[5]) + (v[6] + v[7]));
  const float mean = wave_sum(s) * (1.0f / DM);
  float q0 = 0.f, q1 = 0.f;
#pragma unroll
  for (int i = 0; i < 8; i += 2) { const float d0 = v[i] - mean, d1 = v[i + 1] - mean; q0 = fmaf(d0, d0, q0); q1 = fmaf(d1, d1, q1); }
  const float var = wave_sum(q0 + q1) * (1.0f / DM);
  const float mb = bfr(mean);
  const float den = bfr(__builtin_amdgcn_sqrtf(bfr(bfr(var) + 1e-5f)));
  const float rc = __builtin_amdgcn_rcpf(den);
#pragma unroll
  for (int i = 0; i < 8; ++i) v[i] = bfr(divr(bfr(v[i] - mb), den, rc));
}
__device__ __forceinline__ u32x4 pack8(const float (&v)[8]) {
  u32x4 o;
#pragma unroll
  for (int i = 0; i < 4; ++i) o[i] = (__float_as_uint(v[2 * i]) >> 16) | (__float_as_uint(v[2 * i + 1]) & 0xffff0000u);
  return o;
}
#define PIN8(a, k) asm volatile("" : "+v"(a[k]), "+v"(a[k + 1]), "+v"(a[k + 2]), "+v"(a[k + 3]), "+v"(a[k + 4]), "+v"(a[k + 5]), "+v"(a[k + 6]), "+v"(a[k + 7]))

__device__ __forceinline__ float softmax_step(float (&x)[16], float& m, float& l, float (&acc)[32], bool b0s, bool b1s, bool b2s, bool b3s) {
  constexpr float SCALE = 0.088388347648318440f, L2E = 1.4426950408889634f;
#pragma unroll
  for (int h = 0; h < 8; ++h) x[h] = trsel<XOR8>(x[h], x[h + 8], b3s);
#pragma unroll
  for (int h = 0; h < 4; ++h) x[h] = trsel<XOR7>(x[h], x[h + 4], b2s);
#pragma unroll
  for (int h = 0; h < 2; ++h) x[h] = trsel<XOR2>(x[h], x[h + 2], b1s);
  x[0] = trsel<XOR1>(x[0], x[1], b0s);
  const float sc = bfr(bfr(x[0]) * SCALE) * L2E;
  float pm = fmaxf(sc, dpp<ROR4>(sc));
  pm = fmaxf(pm, dpp<XOR8>(pm));
  const float mn = fmaxf(m, pm);
  const float alpha = __builtin_amdgcn_exp2f(m - mn), pr = __builtin_amdgcn_exp2f(sc - mn);
  l = fmaf(l, alpha, pr); m = mn;
  const float a0 = dpp<0x00>(alpha), a1 = dpp<0x55>(alpha), a2 = dpp<0xAA>(alpha), a3 = dpp<0xFF>(alpha);
#pragma unroll
  for (int k = 0; k < 8; k += 2) {
    const float al[4] = {a0, a1, a2, a3};
#pragma unroll
    for (int g = 0; g < 4; ++g) {
      f32x2 t = f32x2{acc[8 * g + k], acc[8 * g + k + 1]} * f32x2{al[g], al[g]};
      acc[8 * g + k] = t[0]; acc[8 * g + k + 1] = t[1];
    }
  }
  return pr;
}
template <int T> __device__ __forceinline__ void pv_step(const u32x4& v, float pr, float (&acc)[32]) {
  float vf[8];
#pragma unroll
  for (int k = 0; k < 4; ++k) { vf[2 * k] = bflo(v[k]); vf[2 * k + 1] = bfhi(v[k]); }
  const float p0 = dpp<NEWBC + 4 * T + 0>(pr), p1 = dpp<NEWBC + 4 * T + 1>(pr), p2 = dpp<NEWBC + 4 * T + 2>(pr), p3 = dpp<NEWBC + 4 * T + 3>(pr);
  const float pp[4] = {p0, p1, p2, p3};
#pragma unroll
  for (int k = 0; k < 8; k += 2)
#pragma unroll
    for (int g = 0; g < 4; ++g) {
      const f32x2 t = __builtin_elementwise_fma(f32x2{pp[g], pp[g]}, f32x2{vf[k], vf[k + 1]}, f32x2{acc[8 * g + k], acc[8 * g + k + 1]});
      acc[8 * g + k] = t[0]; acc[8 * g + k + 1] = t[1];
    }
}
__device__ __forceinline__ void qk4(const u32x4& k, const u32x4 (&q)[4], float* x) {
#pragma unroll
  for (int g = 0; g < 4; ++g) {
    float d = 0.f;
#pragma unroll
    for (int i = 0; i < 4; ++i) d = dot2(k[i], q[g][i], d);
    x[g] = d;
  }
}

template <int NP> __device__ __forceinline__ f32x4 proj_tile(const u32x4 (&wt)[2 * NP], const unsigned char* act, int row0, bool b3s) {
  float y[32];
#pragma unroll
  for (int i = 0; i < 32; ++i) y[i] = 0.f;
#pragma unroll
  for (int pp = 0; pp < NP; ++pp) {
    unsigned apk[4];
#pragma unroll
    for (int b = 0; b < 4; ++b) apk[b] = *(const unsigned*)(act + (b * DM + row0 + 16 * pp) * 2);
#pragma unroll
    for (int k = 0; k < 4; ++k) {
      const unsigned lo = __builtin_amdgcn_perm(wt[2 * pp + 1][k], wt[2 * pp][k], 0x05040100u);
      const unsigned hi = __builtin_amdgcn_perm(wt[2 * pp + 1][k], wt[2 * pp][k], 0x07060302u);
#pragma unroll
      for (int b = 0; b < 4; ++b) { y[b * 8 + 2 * k] = dot2(lo, apk[b], y[b * 8 + 2 * k]); y[b * 8 + 2 * k + 1] = dot2(hi, apk[b], y[b * 8 + 2 * k + 1]); }
    }
  }
#pragma unroll
  for (int h = 0; h < 16; ++h) y[h] = swadd32(y[h], y[h + 16]);
#pragma unroll
  for (int h = 0; h < 8; ++h) y[h] = swadd16(y[h], y[h + 8]);
#pragma unroll
  for (int h = 0; h < 4; ++h) y[h] = trsel<XOR8>(y[h], y[h + 4], b3s);
  return f32x4{y[0], y[1], y[2], y[3]};
}
__device__ __forceinline__ void lds_arrive(LAS unsigned* cnt, int lane) {
  asm volatile("s_waitcnt lgkmcnt(0)" ::: "memory");
  if (lane == 0) __hip_atomic_fetch_add(cnt, 1u, __ATOMIC_RELAXED, __HIP_MEMORY_SCOPE_WORKGROUP);
}
template <int SLEEP> __device__ __forceinline__ void lds_wait(LAS unsigned* cnt, unsigned target) {
#pragma nounroll
  for (unsigned spins = 0; spins < (1u << 24); ++spins) {
    const unsigned v = __builtin_amdgcn_readfirstlane(__hip_atomic_load(cnt, __ATOMIC_RELAXED, __HIP_MEMORY_SCOPE_WORKGROUP));
    if (v >= target) break;
    if (SLEEP) __builtin_amdgcn_s_sleep(SLEEP);
  }
  asm volatile("" ::: "memory");
}

__global__ void __launch_bounds__(NT, 4) fused_decode_kernel(Params p) {
  __shared__ __attribute__((aligned(16))) unsigned char lds[L_END];
  const int bid = blockIdx.x, tid = threadIdx.x, lane = tid & 63;
  const int w = __builtin_amdgcn_readfirstlane(tid >> 6);
  const int r = lane >> 4, c = lane & 15;
  const int xg = bid & 7, yg = bid >> 3;
  STAMP(0);
  LAS unsigned* sync_cnt = (LAS unsigned*)(lds + L_TAG + 8);
  LAS unsigned* q_cnt = (LAS unsigned*)(lds + L_TAG + 12);
  LAS unsigned* tk_take = (LAS unsigned*)(lds + L_TAG + 16);
  LAS unsigned* tk_give = (LAS unsigned*)(lds + L_TAG + 20);
  LAS unsigned* o_flag = (LAS unsigned*)(lds + L_TAG + 24);
  LAS unsigned* rem_cnt = (LAS unsigned*)(lds + L_TAG + 36);
#define TICKET() do { unsigned tk_ = 0; \
    if (lane == 0) tk_ = __hip_atomic_fetch_add(tk_take, 1u, __ATOMIC_RELAXED, __HIP_MEMORY_SCOPE_WORKGROUP); \
    tk_ = __builtin_amdgcn_readfirstlane(tk_); \
    lds_wait<1>(tk_give, (unsigned)max(0, (int)tk_ + 1 - WINDOW)); __builtin_amdgcn_sched_barrier(0); } while (0)
#define LANDED() do { asm volatile("" :: "v"(kr[0]), "v"(kr[1]), "v"(kr[2]), "v"(kr[3])); \
    if (lane == 0) __hip_atomic_fetch_add(tk_give, 1u, __ATOMIC_RELAXED, __HIP_MEMORY_SCOPE_WORKGROUP); __builtin_amdgcn_sched_barrier(0); } while (0)
  const __amdgpu_buffer_rsrc_t rws = __builtin_amdgcn_make_buffer_rsrc((void*)p.ws, 0, WS_END, RSRC_W3);
  const __amdgpu_buffer_rsrc_t rx = __builtin_amdgcn_make_buffer_rsrc((void*)p.x, 0, NB * DM * 2, RSRC_W3);
  const int ab = yg >> 3, akv = xg, asp = yg & 7;
  const size_t kvbase = (size_t)(ab * HKV + akv) * SK * DH;
  const __amdgpu_buffer_rsrc_t rk = __builtin_amdgcn_make_buffer_rsrc((void*)(p.kc + kvbase), 0, PAGES * 4096, RSRC_W3);
  const __amdgpu_buffer_rsrc_t rv = __builtin_amdgcn_make_buffer_rsrc((void*)(p.vc + kvbase), 0, PAGES * 4096, RSRC_W3);
  const int voff = lane * 16;
#define PAGE_OF(i_) ((((((int)(i_)) >> 4) + ab * RB + akv * RA) & 7) * 128 + asp * 16 + (((int)(i_)) & 15))
  const int i0 = w < 12 ? w - 4 : 120 + 2 * (w - 12), istep = w < 12 ? 8 : 1;
  const unsigned iend = w < 12 ? 120u : (unsigned)(i0 + 2);
  const bool b0s = lane & 1, b1s = lane & 2, b2s = lane & 4, b3s = lane & 8;
  const int oh = yg, onc = xg;
  const int okc = akv * 8 + asp;
#define LOAD_PAGE(so_) do { \
    _Pragma("unroll") for (int t = 0; t < 4; ++t) kr[t] = __builtin_amdgcn_raw_buffer_load_b128(rk, voff + t * 1024, (so_), AUX_NT); \
    _Pragma("unroll") for (int t = 0; t < 4; ++t) vr[t] = __builtin_amdgcn_raw_buffer_load_b128(rv, voff + t * 1024, VOFF(so_), AUX_NT); } while (0)
#define MAKE_TAG(e0_, e1_) do { \
    epoch = ((u64)__builtin_amdgcn_readfirstlane(e1_) << 32) | (u64)__builtin_amdgcn_readfirstlane(e0_); \
    const u64 tg_ = ((epoch + 1ull) * 0x9E3779B97F4A7C15ull) >> 24; tlo = (unsigned)(tg_ & 0xffu) << 24; thi = (unsigned)(tg_ >> 8); } while (0)

  u32x4 kr[4], vr[4];
  unsigned tlo, thi;
  u64 epoch;
  if (w < 4) {
    __builtin_amdgcn_s_setprio(3);
    const int cn = lane & 7, rp = lane >> 3;
    const int qrow = (yg >> 3) * 128 + w * 32 + 2 * rp, qcol = xg * 512 + (yg & 7) * 64;
    const int hrow = xg * 64 + w * 16 + 2 * rp, hcol = yg * 64;
    const bool has_mlp = yg < 28;
    if (tid == 0) { *sync_cnt = 0u; *q_cnt = 0u; *tk_take = W0; *tk_give = 0u; *o_flag = 0u; *rem_cnt = 0u; }
    const u32x4 xa = __builtin_amdgcn_raw_buffer_load_b128(rx, (w * DM + lane * 8) * 2, 0, 0);
    u32x4 wqv[4], w1v[2];
    {
      const __amdgpu_buffer_rsrc_t rq = __builtin_amdgcn_make_buffer_rsrc((void*)p.wq, 0, DM * 4096 * 2, RSRC_W3);
      const int o = (qrow * 4096 + qcol + cn * 8) * 2;
      wqv[0] = __builtin_amdgcn_raw_buffer_load_b128(rq, o, 0, AUX_NT);
      wqv[1] = __builtin_amdgcn_raw_buffer_load_b128(rq, o + 8192, 0, AUX_NT);
      wqv[2] = __builtin_amdgcn_raw_buffer_load_b128(rq, o + 16 * 8192, 0, AUX_NT);
      wqv[3] = __builtin_amdgcn_raw_buffer_load_b128(rq, o + 17 * 8192, 0, AUX_NT);
    }
    const u32x2 e = __builtin_amdgcn_raw_buffer_load_b64(rws, WS_EPOCH, 0, AUX_SC1);
    {
      const __amdgpu_buffer_rsrc_t r1s = __builtin_amdgcn_make_buffer_rsrc((void*)p.w1, 0, DM * FF * 2, RSRC_W3);
      const int o = has_mlp ? (hrow * FF + hcol + cn * 8) * 2 : OOB;
      w1v[0] = __builtin_amdgcn_raw_buffer_load_b128(r1s, o, 0, AUX_NT);
      w1v[1] = __builtin_amdgcn_raw_buffer_load_b128(r1s, o + FF * 2, 0, AUX_NT);
    }
    __builtin_amdgcn_sched_barrier(0);
    LDS_BARRIER();
    __builtin_amdgcn_sched_barrier(0);
    STAMP(1);
    float lnv[8];
#pragma unroll
    for (int i = 0; i < 4; ++i) { lnv[2 * i] = bflo(xa[i]); lnv[2 * i + 1] = bfhi(xa[i]); }
    layernorm8(lnv);
    *(u32x4*)(lds + L_XN + (w * DM + lane * 8) * 2) = pack8(lnv);
    lds_arrive(sync_cnt, lane);
    lds_wait<0>(sync_cnt, 4);
    STAMP(2);
    {
      const f32x4 y = proj_tile<2>(wqv, lds + L_XN, qrow, b3s);
      *(f32x4*)(lds + L_RQ + ((w * 4 + r) * 64 + cn * 8 + (b3s ? 4 : 0)) * 4) = y;
    }
    MAKE_TAG(e[0], e[1]);
    if (tid == 0) *(u32x2*)(lds + L_TAG) = e;
    lds_arrive(sync_cnt, lane);
    lds_wait<0>(sync_cnt, 8);
    STAMP(3);
    {
      const float* rq = (const float*)(lds + L_RQ) + w * 64 + lane;
      const float sq = (rq[0] + rq[256]) + (rq[512] + rq[768]);
      __builtin_amdgcn_raw_buffer_store_b64(gpack(sq, tlo, thi), rws, WS_QG + (((yg >> 3) * NB + w) * 4096 + qcol + lane) * 8, 0, AUX_SC1);
    }
    STAMP(4);
    layernorm8(lnv);
    *(u32x4*)(lds + L_Y + (w * DM + lane * 8) * 2) = pack8(lnv);
    lds_arrive(sync_cnt, lane);
    lds_wait<0>(sync_cnt, 12);
    {
      const f32x4 y = proj_tile<1>(w1v, lds + L_Y, hrow, b3s);
      *(f32x4*)(lds + L_RH + ((w * 4 + r) * 64 + cn * 8 + (b3s ? 4 : 0)) * 4) = y;
    }
    lds_arrive(sync_cnt, lane);
    u32x4 qg[4];
    const int qgo = WS_QG + (ab * 4096 + akv * 512 + w * 128) * 8 + lane * 16;
    constexpr int QSTR = NB * 4096 * 8;
#pragma unroll
    for (int i = 0; i < 4; ++i) qg[i] = __builtin_amdgcn_raw_buffer_load_b128(rws, qgo + i * QSTR, 0, AUX_SC1);
    __builtin_amdgcn_sched_barrier(0);
    lds_wait<0>(sync_cnt, 16);
    {
      const float* rh = (const float*)(lds + L_RH) + w * 64 + lane;
      const float sh = (rh[0] + rh[256]) + (rh[512] + rh[768]);
      __builtin_amdgcn_raw_buffer_store_b64(gpack(sh, tlo, thi), rws, has_mlp ? WS_HG + ((xg * NB + w) * FF + hcol + lane) * 8 : OOB, 0, AUX_SC1);
    }
    STAMP(5);
    for (unsigned spins = 0;; ++spins) {
      unsigned bad = 0;
#pragma unroll
      for (int i = 0; i < 4; ++i) bad |= gbad(qg[i][0], qg[i][1], tlo, thi) | gbad(qg[i][2], qg[i][3], tlo, thi);
      if (__builtin_amdgcn_ballot_w64(bad != 0) == 0ull || spins > SPIN_CAP) break;
      STAMP(13);
      asm volatile("" ::: "memory");
#pragma unroll
      for (int i = 0; i < 4; ++i) qg[i] = __builtin_amdgcn_raw_buffer_load_b128(rws, qgo + i * QSTR, 0, AUX_SC1);
    }
    {
      const float q0 = (gval(qg[0][0]) + gval(qg[1][0])) + (gval(qg[2][0]) + gval(qg[3][0]));
      const float q1 = (gval(qg[0][2]) + gval(qg[1][2])) + (gval(qg[2][2]) + gval(qg[3][2]));
      ((unsigned*)(lds + L_Q))[w * 64 + lane] = bfbits(q0) | (bfbits(q1) << 16);
    }
    lds_arrive(q_cnt, lane);
    __builtin_amdgcn_s_setprio(0);
#pragma unroll
    for (int t = 0; t < 4; ++t) { kr[t] = u32x4{0u, 0u, 0u, 0u}; vr[t] = u32x4{0u, 0u, 0u, 0u}; }
  } else {
    if (w >= 4 + W0) {
      __builtin_amdgcn_s_barrier();
      asm volatile("" ::: "memory");
      TICKET();
    }
    LOAD_PAGE(PAGE_OF(i0) * 4096);
    __builtin_amdgcn_sched_barrier(0);
    if (w < 4 + W0) {
      __builtin_amdgcn_s_barrier();
      asm volatile("" ::: "memory");
    }
    const int t2 = tid - 768, kp = t2 >> 3, cn = t2 & 7;
    const __amdgpu_buffer_rsrc_t rw2 = __builtin_amdgcn_make_buffer_rsrc((void*)p.w2, 0, FF * DM * 2, RSRC_W3);
    const int oo = (w >= 12 && kp < 28) ? ((oh * 56 + 2 * kp) * DM + onc * 64 + cn * 8) * 2 : OOB;
    const u32x4 ua = __builtin_amdgcn_raw_buffer_load_b128(rw2, oo, 0, AUX_NT);
    const u32x4 ub = __builtin_amdgcn_raw_buffer_load_b128(rw2, oo + DM * 2, 0, AUX_NT);
    __builtin_amdgcn_sched_barrier(0);
    STAMP(1);
    LANDED();
    unsigned char* pk = lds + L_PARK + (w - 4) * 8192 + voff;
#pragma unroll
    for (int t = 0; t < 4; ++t) *(u32x4*)(pk + t * 1024) = kr[t];
#pragma unroll
    for (int t = 0; t < 4; ++t) *(u32x4*)(pk + 4096 + t * 1024) = vr[t];
    __builtin_amdgcn_sched_barrier(0);
    STAMP(2);
    if (w >= 12) {
      unsigned char* dst = lds + L_W2T + cn * 144 + kp * 4;
#pragma unroll
      for (int k = 0; k < 4; ++k) {
        *(unsigned*)(dst + (2 * k) * 8 * 144) = __builtin_amdgcn_perm(ub[k], ua[k], 0x05040100u);
        *(unsigned*)(dst + (2 * k + 1) * 8 * 144) = __builtin_amdgcn_perm(ub[k], ua[k], 0x07060302u);
      }
    }
    asm volatile("" :: "v"(ua), "v"(ub));
    TICKET();
    LOAD_PAGE(PAGE_OF(i0 + istep) * 4096);
    __builtin_amdgcn_sched_barrier(0);
    STAMP(3);
    LANDED();
  }
  lds_wait<2>(q_cnt, 4);
  {
    const u32x2 ep = *(const u32x2*)(lds + L_TAG);
    MAKE_TAG(ep[0], ep[1]);
  }
  STAMP(6);

  float m = -INFINITY, l = 0.f, acc[32];
#pragma unroll
  for (int k = 0; k < 32; ++k) acc[k] = 0.f;
  if (w >= 4) {
    u32x4 q[4];
#pragma unroll
    for (int g = 0; g < 4; ++g) q[g] = *(const u32x4*)(lds + L_Q + g * 256 + c * 16);
    unsigned nxt = (unsigned)(i0 + 2 * istep);
    unsigned parkat = nxt;
    asm volatile("" : "+v"(parkat));
    parkat = __builtin_amdgcn_readfirstlane(parkat);
    bool more;
    do {
      float x[16];
#pragma unroll
      for (int t = 0; t < 4; ++t) qk4(kr[t], q, x + 4 * t);
      PIN8(x, 0); PIN8(x, 8);
      more = nxt < iend;
      const int so = more ? PAGE_OF(nxt) * 4096 : OOB;
      __builtin_amdgcn_sched_barrier(0);
#pragma unroll
      for (int t = 0; t < 4; ++t) kr[t] = __builtin_amdgcn_raw_buffer_load_b128(rk, voff + t * 1024, so, AUX_NT);
      __builtin_amdgcn_sched_barrier(0);
      const float pr = softmax_step(x, m, l, acc, b0s, b1s, b2s, b3s);
      pv_step<0>(vr[0], pr, acc); pv_step<1>(vr[1], pr, acc); pv_step<2>(vr[2], pr, acc); pv_step<3>(vr[3], pr, acc);
      PIN8(acc, 0); PIN8(acc, 8); PIN8(acc, 16); PIN8(acc, 24);
      __builtin_amdgcn_sched_barrier(0);
#pragma unroll
      for (int t = 0; t < 4; ++t) vr[t] = __builtin_amdgcn_raw_buffer_load_b128(rv, voff + t * 1024, more ? VOFF(so) : OOB, AUX_NT);
      __builtin_amdgcn_sched_barrier(0);
      if (istep == 8) {
        unsigned old = 0;
        if (lane == 0) old = __hip_atomic_fetch_add(rem_cnt, 1u, __ATOMIC_RELAXED, __HIP_MEMORY_SCOPE_WORKGROUP);
        old = __builtin_amdgcn_readfirstlane(old);
        if (nxt - 16u > old) __builtin_amdgcn_s_setprio(0); else __builtin_amdgcn_s_setprio(1);
      }
      if (nxt == parkat) {
        const unsigned char* pk = lds + L_PARK + (w - 4) * 8192 + voff;
        float y[16];
#pragma unroll
        for (int t = 0; t < 4; ++t) { const u32x4 kk = *(const u32x4*)(pk + t * 1024); qk4(kk, q, y + 4 * t); }
        const float p2 = softmax_step(y, m, l, acc, b0s, b1s, b2s, b3s);
        { const u32x4 vv = *(const u32x4*)(pk + 4096); pv_step<0>(vv, p2, acc); }
        { const u32x4 vv = *(const u32x4*)(pk + 4096 + 1024); pv_step<1>(vv, p2, acc); }
        { const u32x4 vv = *(const u32x4*)(pk + 4096 + 2048); pv_step<2>(vv, p2, acc); }
        { const u32x4 vv = *(const u32x4*)(pk + 4096 + 3072); pv_step<3>(vv, p2, acc); }
        PIN8(acc, 0); PIN8(acc, 8); PIN8(acc, 16); PIN8(acc, 24);
        __builtin_amdgcn_sched_barrier(0);
      }
      nxt += istep;
    } while (more);
  } else {
    m = -1e30f;
  }
  STAMP(7);
  const bool is_op = w >= 4 && w < 12;
  {
    const __amdgpu_buffer_rsrc_t ro = __builtin_amdgcn_make_buffer_rsrc((void*)p.wo, 0, 4096 * DM * 2, RSRC_W3);
    const int vo = is_op ? (lane >> 3) * 2048 + (lane & 7) * 16 : OOB;
    const int so = okc * 65536 + (w - 4) * 128;
#pragma unroll
    for (int t = 0; t < 4; ++t) {
      kr[t] = __builtin_amdgcn_raw_buffer_load_b128(ro, vo, so + t * 16384, WO_AUX);
      vr[t] = __builtin_amdgcn_raw_buffer_load_b128(ro, vo + 1024, so + t * 16384, WO_AUX);
    }
  }
  __builtin_amdgcn_sched_barrier(0);

  {
    l += dpp<ROR4>(l); l += dpp<XOR8>(l);
    const float M = swmax32(swmax16(m));
    const float f = __builtin_amdgcn_exp2f(m - M);
    l *= f;
    l = swadd16(l, l); l = swadd32(l, l);
    const float a0 = dpp<0x00>(f), a1 = dpp<0x55>(f), a2 = dpp<0xAA>(f), a3 = dpp<0xFF>(f);
#pragma unroll
    for (int k = 0; k < 8; ++k) { acc[k] *= a0; acc[8 + k] *= a1; acc[16 + k] *= a2; acc[24 + k] *= a3; }
#pragma unroll
    for (int k = 0; k < 16; ++k) acc[k] = swadd32(acc[k], acc[k + 16]);
#pragma unroll
    for (int k = 0; k < 8; ++k) acc[k] = swadd16(acc[k], acc[k + 8]);
    float* rec = (float*)(lds + (w < 12 ? L_CM + w * 2048 : L_PARK + (w - 4) * 8192)) + r * 128 + 8 * c;
    *(f32x4*)rec = f32x4{acc[0], acc[1], acc[2], acc[3]};
    *(f32x4*)(rec + 4) = f32x4{acc[4], acc[5], acc[6], acc[7]};
    if (lane < 4) { ((float*)(lds + L_MM))[lane * 16 + w] = M; ((float*)(lds + L_LL))[lane * 16 + w] = l; }
  }
  if (is_op) {
    asm volatile("s_waitcnt lgkmcnt(0)" ::: "memory");
    __builtin_amdgcn_sched_barrier(0);
    __builtin_amdgcn_s_barrier();
    __builtin_amdgcn_sched_barrier(0);
    asm volatile("" : "+v"(kr[0]), "+v"(kr[1]), "+v"(kr[2]), "+v"(kr[3]), "+v"(vr[0]), "+v"(vr[1]), "+v"(vr[2]), "+v"(vr[3]) :: "memory");
    unsigned char* slot = lds + L_PARK + (w - 4) * 8192;
    const int kq = lane >> 3, cn = lane & 7;
#pragma unroll
    for (int t = 0; t < 4; ++t)
#pragma unroll
      for (int j = 0; j < 8; ++j) {
        const int ch = (2 * t + (kq >> 2)) ^ ((4 * j + (cn >> 1)) & 7);
        *(unsigned*)(slot + (j * 8 + cn) * 128 + ch * 16 + (kq & 3) * 4) =
            __builtin_amdgcn_perm(vr[t][j >> 1], kr[t][j >> 1], (j & 1) ? 0x07060302u : 0x05040100u);
      }
    asm volatile("s_waitcnt lgkmcnt(0)" ::: "memory");
    u32x4 wc[8];
#pragma unroll
    for (int i = 0; i < 8; ++i) wc[i] = *(const u32x4*)(slot + lane * 128 + ((i ^ ((lane >> 1) & 7)) << 4));
    const int so = WS_SG + ((okc * NB + ab) * DM + (w - 4) * 64 + 8 * (lane & 7) + (lane >> 3)) * 8;
    asm volatile("" :: "v"(wc[0]), "v"(wc[1]), "v"(wc[2]), "v"(wc[3]), "v"(wc[4]), "v"(wc[5]), "v"(wc[6]), "v"(wc[7]), "v"(so));
    lds_wait<1>(o_flag, 1);
    u32x4 ov[8];
#pragma unroll
    for (int i = 0; i < 8; ++i) ov[i] = *(const u32x4*)(lds + L_O + 512 + i * 16);
    __builtin_amdgcn_sched_barrier(0);
    float s0 = 0.f, s1 = 0.f;
#pragma unroll
    for (int i = 0; i < 8; i += 2)
#pragma unroll
      for (int k = 0; k < 4; ++k) { s0 = dot2(wc[i][k], ov[i][k], s0); s1 = dot2(wc[i + 1][k], ov[i + 1][k], s1); }
    __builtin_amdgcn_raw_buffer_store_b64(gpack(s0 + s1, tlo, thi), rws, so, 0, AUX_SC1);
    STAMP(11);
  } else {
    LDS_BARRIER();
    STAMP(8);
    {
      const int mi = w < 4 ? w : w - 8, g = mi >> 1, e = mi * 64 + lane, d = e & 127;
      f32x4 mv[4], lv[4];
      float rv16[16];
#pragma unroll
      for (int i = 0; i < 4; ++i) mv[i] = *(const f32x4*)(lds + L_MM + g * 64 + i * 16);
#pragma unroll
      for (int i = 0; i < 16; ++i) rv16[i] = *(const float*)(lds + (i < 12 ? L_CM + i * 2048 : L_PARK + (i - 4) * 8192) + e * 4);
#pragma unroll
      for (int i = 0; i < 4; ++i) lv[i] = *(const f32x4*)(lds + L_LL + g * 64 + i * 16);
      __builtin_amdgcn_sched_barrier(0);
      float M = -INFINITY;
#pragma unroll
      for (int i = 0; i < 16; ++i) M = fmaxf(M, mv[i >> 2][i & 3]);
      M = __uint_as_float(__float_as_uint(M) & 0xffffff00u);
      float L = 0.f, o = 0.f;
#pragma unroll
      for (int i = 0; i < 16; ++i) {
        const float wt = __builtin_amdgcn_exp2f(mv[i >> 2][i & 3] - M);
        L = fmaf(wt, lv[i >> 2][i & 3], L);
        o = fmaf(wt, rv16[i], o);
      }
      const int rec = (ab * HQ + akv * 4 + g) * 8 + asp;
      __builtin_amdgcn_raw_buffer_store_b64(gpack(o, tlo, thi), rws, WS_PG + (rec * 128 + d) * 8, 0, AUX_SC1);
      u32x2 ml = gpack(d == 0 ? M : L, tlo, thi);
      if (d == 0) ml[0] = (__float_as_uint(M) >> 8) | tlo;
      __builtin_amdgcn_raw_buffer_store_b64(ml, rws, d < 2 ? WS_ML + (rec * 2 + d) * 8 : OOB, 0, AUX_SC1);
    }
    STAMP(9);
    if (w < 4) {
      const int hu = oh * 56 + (lane < 56 ? lane : 0);
      const int ho = WS_HG + (w * FF + hu) * 8;
      constexpr int HSTR = NB * FF * 8;
      const __amdgpu_buffer_rsrc_t rb = __builtin_amdgcn_make_buffer_rsrc((void*)p.b1, 0, FF * 2, RSRC_W3);
      const unsigned bb = __builtin_amdgcn_raw_buffer_load_b16(rb, hu * 2, 0, 0);
      u32x2 hg[8];
#pragma unroll
      for (int i = 0; i < 8; ++i) hg[i] = __builtin_amdgcn_raw_buffer_load_b64(rws, ho + i * HSTR, 0, AUX_SC1);
      __builtin_amdgcn_sched_barrier(0);
      u32x4 wc[7];
#pragma unroll
      for (int i = 0; i < 7; ++i) wc[i] = *(const u32x4*)(lds + L_W2T + lane * 144 + i * 16);
#pragma nounroll
      for (unsigned spins = 0;; ++spins) {
        unsigned bad = 0;
#pragma unroll
        for (int i = 0; i < 8; ++i) bad |= gbad(hg[i][0], hg[i][1], tlo, thi);
        if (__builtin_amdgcn_ballot_w64(bad != 0) == 0ull || spins >= SPIN_CAP) break;
        __builtin_amdgcn_s_sleep(8);
        asm volatile("" ::: "memory");
#pragma unroll
        for (int i = 0; i < 8; ++i) hg[i] = __builtin_amdgcn_raw_buffer_load_b64(rws, ho + i * HSTR, 0, AUX_SC1);
      }
      const float sh = ((gval(hg[0][0]) + gval(hg[1][0])) + (gval(hg[2][0]) + gval(hg[3][0]))) +
                       ((gval(hg[4][0]) + gval(hg[5][0])) + (gval(hg[6][0]) + gval(hg[7][0])));
      const float u = bfr(bfr(sh) + bflo(bb));
      const float z = 0.7978845608028654f * (u + 0.044715f * u * u * u);
      const float th = 1.0f - 2.0f * __builtin_amdgcn_rcpf(__expf(2.0f * z) + 1.0f);
      u16* ob = (u16*)(lds + L_O + w * 128);
      ob[lane] = (u16)bfbits(0.5f * u * (1.0f + th));
      asm volatile("s_waitcnt lgkmcnt(0)" ::: "memory");
      u32x4 ov[7];
#pragma unroll
      for (int i = 0; i < 7; ++i) ov[i] = *(const u32x4*)(lds + L_O + w * 128 + i * 16);
      __builtin_amdgcn_sched_barrier(0);
      float sm = 0.f;
#pragma unroll
      for (int i = 0; i < 7; ++i)
#pragma unroll
        for (int k = 0; k < 4; ++k) sm = dot2(wc[i][k], ov[i][k], sm);
      __builtin_amdgcn_raw_buffer_store_b64(gpack(sm, tlo, thi), rws,
                                            WS_MG + ((oh * NB + w) * DM + onc * 64 + 8 * (lane & 7) + (lane >> 3)) * 8, 0, AUX_SC1);
    } else if (w == 12) {
      const int sp = lane >> 3, dd = lane & 7;
      const int crec = (ab * HQ + (okc >> 1)) * 8 + sp;
      const int po = WS_PG + (crec * 128 + (okc & 1) * 64) * 8 + dd * 16, mo = WS_ML + crec * 16;
      u32x4 pa[4], ma, pgb[4], mgb;
      float gv[10];
#define G_TAKE(P, M) do { gv[8] = gval(M[0]); gv[9] = gval(M[2]); \
        _Pragma("unroll") for (int i = 0; i < 4; ++i) { gv[2 * i] = gval(P[i][0]); gv[2 * i + 1] = gval(P[i][2]); } \
        asm volatile("" : "+v"(gv[0]), "+v"(gv[1]), "+v"(gv[2]), "+v"(gv[3]), "+v"(gv[4]), "+v"(gv[5]), "+v"(gv[6]), "+v"(gv[7]), "+v"(gv[8]), "+v"(gv[9])); } while (0)
#define G_LOAD(P, M) do { M = __builtin_amdgcn_raw_buffer_load_b128(rws, mo, 0, AUX_SC1); \
        _Pragma("unroll") for (int i = 0; i < 4; ++i) P[i] = __builtin_amdgcn_raw_buffer_load_b128(rws, po + i * 128, 0, AUX_SC1); \
        __builtin_amdgcn_sched_barrier(0); asm volatile("" ::: "memory"); } while (0)
#define G_OK(P, M) (__builtin_amdgcn_ballot_w64((gbad(M[0], M[1], tlo, thi) | gbad(M[2], M[3], tlo, thi) | \
        gbad(P[0][0], P[0][1], tlo, thi) | gbad(P[0][2], P[0][3], tlo, thi) | gbad(P[1][0], P[1][1], tlo, thi) | gbad(P[1][2], P[1][3], tlo, thi) | \
        gbad(P[2][0], P[2][1], tlo, thi) | gbad(P[2][2], P[2][3], tlo, thi) | gbad(P[3][0], P[3][1], tlo, thi) | gbad(P[3][2], P[3][3], tlo, thi)) != 0) == 0ull)
      G_LOAD(pgb, mgb);
      __builtin_amdgcn_s_sleep(GSTAG);
#pragma nounroll
      for (unsigned spins = 0;; ++spins) {
        G_LOAD(pa, ma);
        G_TAKE(pgb, mgb);
        if (G_OK(pgb, mgb)) break;
        G_LOAD(pgb, mgb);
        G_TAKE(pa, ma);
        if (G_OK(pa, ma) || spins >= SPIN_CAP) break;
      }
      const bool done = false;
      if (!done) {
        STAMP(10);
        const float ms = gv[8], ls = gv[9];
        float Mx = fmaxf(ms, dpp<XOR8>(ms));
        Mx = swmax32(swmax16(Mx));
        const float wt = __builtin_amdgcn_exp2f(ms - Mx);
        float Ls = wt * ls;
        Ls += dpp<XOR8>(Ls); Ls = swadd16(Ls, Ls); Ls = swadd32(Ls, Ls);
        float v[8];
#pragma unroll
        for (int i = 0; i < 8; ++i) v[i] = wt * gv[i];
#pragma unroll
        for (int i = 0; i < 4; ++i) v[i] = swadd32(v[i], v[i + 4]);
#pragma unroll
        for (int i = 0; i < 2; ++i) v[i] = swadd16(v[i], v[i + 2]);
        v[0] = trsel<XOR8>(v[0], v[1], b3s);
        const int dim = 2 * dd + ((lane >> 3) & 1) + 16 * (lane >> 4);
        ((u16*)(lds + L_O + 512))[dim] = (u16)bfbits(v[0] * __builtin_amdgcn_rcpf(Ls));
        asm volatile("s_waitcnt lgkmcnt(0)" ::: "memory");
        if (lane == 0) __hip_atomic_store(o_flag, 1u, __ATOMIC_RELAXED, __HIP_MEMORY_SCOPE_WORKGROUP);
      }
      __builtin_amdgcn_sched_barrier(0);
      asm volatile("" :: "v"(pa[0]), "v"(pa[1]), "v"(pa[2]), "v"(pa[3]), "v"(ma), "v"(pgb[0]), "v"(pgb[1]), "v"(pgb[2]), "v"(pgb[3]), "v"(mgb));
    } else if (w == 15) {
      const int fb = bid >> 6, n0 = (bid & 63) * 8, kq = lane >> 2, p2 = lane & 3;
      const int so = WS_SG + ((kq * NB + fb) * DM + n0 + 2 * p2) * 8, mo = WS_MG + ((kq * NB + fb) * DM + n0 + 2 * p2) * 8;
      constexpr int KSTR = 16 * NB * DM * 8;
      const __amdgpu_buffer_rsrc_t rb = __builtin_amdgcn_make_buffer_rsrc((void*)p.b2, 0, DM * 2, RSRC_W3);
      const unsigned b2v = __builtin_amdgcn_raw_buffer_load_b32(rb, (n0 + 2 * p2) * 2, 0, 0);
      const unsigned xv = __builtin_amdgcn_raw_buffer_load_b32(rx, (fb * DM + n0 + 2 * p2) * 2, 0, 0);
      u32x4 sg[6], sb[4];
#pragma nounroll
      for (unsigned spins = 0;; ++spins) {
#pragma unroll
        for (int i = 0; i < 2; ++i) sg[4 + i] = __builtin_amdgcn_raw_buffer_load_b128(rws, mo + i * KSTR, 0, AUX_SC1);
        const unsigned bad = gbad(sg[4][0], sg[4][1], tlo, thi) | gbad(sg[4][2], sg[4][3], tlo, thi) |
                             gbad(sg[5][0], sg[5][1], tlo, thi) | gbad(sg[5][2], sg[5][3], tlo, thi);
        if (__builtin_amdgcn_ballot_w64(bad != 0) == 0ull || spins >= SPIN_CAP) break;
        __builtin_amdgcn_s_sleep(PSLEEP);
        asm volatile("" ::: "memory");
      }
      lds_wait<2>(o_flag, 1);
#define F_LOAD(P) do { _Pragma("unroll") for (int i = 0; i < 4; ++i) P[i] = __builtin_amdgcn_raw_buffer_load_b128(rws, so + i * KSTR, 0, AUX_SC1); \
        __builtin_amdgcn_sched_barrier(0); asm volatile("" ::: "memory"); } while (0)
#define F_OK(P) (__builtin_amdgcn_ballot_w64(( \
        gbad(P[0][0], P[0][1], tlo, thi) | gbad(P[0][2], P[0][3], tlo, thi) | gbad(P[1][0], P[1][1], tlo, thi) | gbad(P[1][2], P[1][3], tlo, thi) | \
        gbad(P[2][0], P[2][1], tlo, thi) | gbad(P[2][2], P[2][3], tlo, thi) | gbad(P[3][0], P[3][1], tlo, thi) | gbad(P[3][2], P[3][3], tlo, thi)) != 0) == 0ull)
      F_LOAD(sb);
      __builtin_amdgcn_s_sleep(GSTAG);
#pragma nounroll
      for (unsigned spins = 0;; ++spins) {
        F_LOAD(sg);
        if (F_OK(sb)) {
#pragma unroll
          for (int i = 0; i < 4; ++i) sg[i] = sb[i];
          break;
        }
        F_LOAD(sb);
        if (F_OK(sg) || spins >= SPIN_CAP) break;
      }
      STAMP(12);
      float a[4];
      a[0] = (gval(sg[0][0]) + gval(sg[1][0])) + (gval(sg[2][0]) + gval(sg[3][0]));
      a[1] = (gval(sg[0][2]) + gval(sg[1][2])) + (gval(sg[2][2]) + gval(sg[3][2]));
      a[2] = gval(sg[4][0]) + gval(sg[5][0]);
      a[3] = gval(sg[4][2]) + gval(sg[5][2]);
#pragma unroll
      for (int i = 0; i < 4; ++i) {
        float t = a[i];
        t += dpp<XOR8>(t); t += dpp<ROR4>(t);
        t = swadd16(t, t); t = swadd32(t, t);
        a[i] = t;
      }
      float o[2];
#pragma unroll
      for (int j = 0; j < 2; ++j) {
        const float sv = bfr(a[j]);
        const float mm = bfr(bfr(a[2 + j]) + (j ? bfhi(b2v) : bflo(b2v)));
        const float g = bfr(mm * __builtin_amdgcn_rcpf(1.0f + __expf(-mm)));
        o[j] = (j ? bfhi(xv) : bflo(xv)) + bfr(sv * g);
      }
      {
        const __amdgpu_buffer_rsrc_t rout = __builtin_amdgcn_make_buffer_rsrc((void*)p.out, 0, NB * DM * 2, RSRC_W3);
        __builtin_amdgcn_raw_buffer_store_b32(bfbits(o[0]) | (bfbits(o[1]) << 16), rout, lane < 4 ? (fb * DM + n0 + 2 * p2) * 2 : OOB, 0, OUT_AUX);
      }
      if (bid == 0 && lane == 0) __hip_atomic_store((GAS u64*)(p.ws + WS_EPOCH), epoch + 1ull, RLX_AGENT);
      STAMP(15);
    }
  }
}

extern "C" void kernel_launch(void* const* d_in, const int* in_sizes, int n_in,
                              void* d_out, int out_size, void* d_ws, size_t ws_size,
                              hipStream_t stream) {
  Params p;
  p.x = (const u16*)d_in[0]; p.kc = (const u16*)d_in[1]; p.vc = (const u16*)d_in[2];
  p.wq = (const u16*)d_in[3]; p.wo = (const u16*)d_in[4];
  p.w1 = (const u16*)d_in[6]; p.b1 = (const u16*)d_in[7]; p.w2 = (const u16*)d_in[8]; p.b2 = (const u16*)d_in[9];
  p.out = (u16*)d_out; p.ws = (unsigned char*)d_ws;
  hipLaunchKernelGGL(fused_decode_kernel, dim3(NWG), dim3(NT), 0, stream, p);
}
```

```cpp
#include <hip/hip_runtime.h>
#include <hip/hip_bf16.h>
#include <stdint.h>

constexpr int WINDOW = 7;
constexpr int W0 = 6;
constexpr int TAU = 452, TA0 = 7600;
constexpr int TAUB = 480, TB0 = 7800;
constexpr int T1 = 1700, TCK = 400, NCK = 3, TVD = 180, LAGP = 2, VOTES = 6;
constexpr int ORGMAX = 400;
constexpr int GDEL = 20, FDEL = 12, GSTAG = 10;

typedef unsigned u32x4 __attribute__((ext_vector_type(4)));
typedef unsigned u32x2 __attribute__((ext_vector_type(2)));
typedef float f32x4 __attribute__((ext_vector_type(4)));
typedef float f32x2 __attribute__((ext_vector_type(2)));
typedef __bf16 bf16x2 __attribute__((ext_vector_type(2)));
typedef unsigned short u16;
typedef unsigned long long u64;
#define GAS __attribute__((address_space(1)))
#define LAS __attribute__((address_space(3)))
#define RLX_AGENT __ATOMIC_RELAXED, __HIP_MEMORY_SCOPE_AGENT

constexpr int NB = 4, DM = 512, HQ = 32, HKV = 8, SK = 16384, DH = 128, FF = 1792;
constexpr int NWG = 256, NW = 16, NT = NW * 64;
constexpr int PAGES = SK / 16;
constexpr int AUX_NT = 2, AUX_SC1 = 16;
constexpr int RSRC_W3 = 0x00020000;
constexpr int WS_EPOCH = 0;
constexpr int WS_LAG = 8192;
constexpr int WS_ORG = 4096;
constexpr int WS_QG = 16384;
constexpr int WS_HG = WS_QG + 4 * NB * 4096 * 8;
constexpr int WS_ML = WS_HG + 8 * NB * FF * 8;
constexpr int WS_PG = WS_ML + NB * HQ * 8 * 32 * 8;
constexpr int WS_SG = WS_PG + NB * HQ * 8 * 128 * 8;
constexpr int WS_MG = WS_SG + 64 * NB * DM * 8;
constexpr int WS_END = WS_MG + 32 * NB * DM * 8;
constexpr int OOB = 0x7ff00000;
constexpr int L_CM = 0;
constexpr int L_PARK = L_CM + 8 * 2048;
constexpr int L_WOT = L_PARK + 12 * 8192;
constexpr int L_W2T = L_WOT + 16384;
constexpr int L_MM = L_W2T + 64 * 144;
constexpr int L_LL = L_MM + 256;
constexpr int L_RQ = L_LL + 256;
constexpr int L_RH = L_RQ + 4096;
constexpr int L_Q = L_RH + 4096;
constexpr int L_O = L_Q + 1024;
constexpr int L_XN = L_O + 8 * 128;
constexpr int L_Y = L_XN + 4096;
constexpr int L_TAG = L_Y + 4096;
constexpr int L_END = L_TAG + 48;
constexpr unsigned SPIN_CAP = 1u << 15;

struct Params {
  const u16* x; const u16* kc; const u16* vc; const u16* wq; const u16* wo;
  const u16* w1; const u16* b1; const u16* w2; const u16* b2;
  u16* out; unsigned char* ws;
};

__device__ __forceinline__ float dot2(unsigned a, unsigned b, float acc) {
  return __builtin_amdgcn_fdot2_f32_bf16(__builtin_bit_cast(bf16x2, a), __builtin_bit_cast(bf16x2, b), acc, false);
}
template <int CTRL> __device__ __forceinline__ float dpp(float x) {
  return __builtin_bit_cast(float, __builtin_amdgcn_mov_dpp(__builtin_bit_cast(int, x), CTRL, 0xf, 0xf, true));
}
constexpr int XOR1 = 0xB1, XOR2 = 0x4E, XOR7 = 0x141, XOR8 = 0x128, ROR4 = 0x124, NEWBC = 0x150;
__device__ __forceinline__ float bfr(float x) { return (float)(__bf16)x; }
__device__ __forceinline__ unsigned bfbits(float x) { return (unsigned)__builtin_bit_cast(u16, (__bf16)x); }
__device__ __forceinline__ float bflo(unsigned u) { return __builtin_bit_cast(float, u << 16); }
__device__ __forceinline__ float bfhi(unsigned u) { return __builtin_bit_cast(float, u & 0xffff0000u); }
__device__ __forceinline__ float swadd32(float a, float b) {
  auto s = __builtin_amdgcn_permlane32_swap(__float_as_uint(a), __float_as_uint(b), false, false);
  return __uint_as_float(s[0]) + __uint_as_float(s[1]);
}
__device__ __forceinline__ float swadd16(float a, float b) {
  auto s = __builtin_amdgcn_permlane16_swap(__float_as_uint(a), __float_as_uint(b), false, false);
  return __uint_as_float(s[0]) + __uint_as_float(s[1]);
}
__device__ __forceinline__ float swmax32(float a) {
  auto s = __builtin_amdgcn_permlane32_swap(__float_as_uint(a), __float_as_uint(a), false, false);
  return fmaxf(__uint_as_float(s[0]), __uint_as_float(s[1]));
}
__device__ __forceinline__ float swmax16(float a) {
  auto s = __builtin_amdgcn_permlane16_swap(__float_as_uint(a), __float_as_uint(a), false, false);
  return fmaxf(__uint_as_float(s[0]), __uint_as_float(s[1]));
}
__device__ __forceinline__ float wave_sum(float v) {
  v += dpp<XOR1>(v); v += dpp<XOR2>(v); v += dpp<XOR7>(v); v += dpp<XOR8>(v);
  v = swadd16(v, v); v = swadd32(v, v);
  return v;
}
template <int CTRL> __device__ __forceinline__ float trsel(float lo, float hi, bool sel) {
  const float t1 = lo + dpp<CTRL>(lo), t2 = hi + dpp<CTRL>(hi);
  return sel ? t2 : t1;
}
__device__ __forceinline__ float expd(float a, float b) { return __builtin_amdgcn_exp2f((a - b) * 1.4426950408889634f); }
__device__ __forceinline__ float divr(float a, float d, float r) {
  const float q = a * r;
  return fmaf(fmaf(-q, d, a), r, q);
}
__device__ __forceinline__ u32x2 gpack(float v, unsigned tlo, unsigned thi) {
  const unsigned b = __float_as_uint(v) + 0x80u;
  return u32x2{(b >> 8) | tlo, thi};
}
__device__ __forceinline__ unsigned gbad(unsigned lo, unsigned hi, unsigned tlo, unsigned thi) {
  return (hi ^ thi) | ((lo ^ tlo) & 0xff000000u);
}
__device__ __forceinline__ float gval(unsigned lo) { return __uint_as_float(lo << 8); }
#define LDS_BARRIER() do { asm volatile("s_waitcnt lgkmcnt(0)" ::: "memory"); __builtin_amdgcn_s_barrier(); asm volatile("" ::: "memory"); } while (0)

__device__ __forceinline__ void layernorm8(float (&v)[8]) {
  const float s = ((v[0] + v[1]) + (v[2] + v[3])) + ((v[4] + v[5]) + (v[6] + v[7]));
  const float mean = wave_sum(s) * (1.0f / DM);
  float q0 = 0.f, q1 = 0.f;
#pragma unroll
  for (int i = 0; i < 8; i += 2) { const float d0 = v[i] - mean, d1 = v[i + 1] - mean; q0 = fmaf(d0, d0, q0); q1 = fmaf(d1, d1, q1); }
  const float var = wave_sum(q0 + q1) * (1.0f / DM);
  const float mb = bfr(mean);
  const float den = bfr(__builtin_amdgcn_sqrtf(bfr(bfr(var) + 1e-5f)));
  const float rc = __builtin_amdgcn_rcpf(den);
#pragma unroll
  for (int i = 0; i < 8; ++i) v[i] = bfr(divr(bfr(v[i] - mb), den, rc));
}
__device__ __forceinline__ u32x4 pack8(const float (&v)[8]) {
  u32x4 o;
#pragma unroll
  for (int i = 0; i < 4; ++i) o[i] = (__float_as_uint(v[2 * i]) >> 16) | (__float_as_uint(v[2 * i + 1]) & 0xffff0000u);
  return o;
}
#define PIN8(a, k) asm volatile("" : "+v"(a[k]), "+v"(a[k + 1]), "+v"(a[k + 2]), "+v"(a[k + 3]), "+v"(a[k + 4]), "+v"(a[k + 5]), "+v"(a[k + 6]), "+v"(a[k + 7]))

__device__ __forceinline__ float softmax_step(float (&x)[16], float& m, float& l, float (&acc)[32], bool b0s, bool b1s, bool b2s, bool b3s) {
  constexpr float SCALE = 0.088388347648318440f;
#pragma unroll
  for (int h = 0; h < 8; ++h) x[h] = trsel<XOR8>(x[h], x[h + 8], b3s);
#pragma unroll
  for (int h = 0; h < 4; ++h) x[h] = trsel<XOR7>(x[h], x[h + 4], b2s);
#pragma unroll
  for (int h = 0; h < 2; ++h) x[h] = trsel<XOR2>(x[h], x[h + 2], b1s);
  x[0] = trsel<XOR1>(x[0], x[1], b0s);
  const float sc = bfr(bfr(x[0]) * SCALE);
  float pm = fmaxf(sc, dpp<ROR4>(sc));
  pm = fmaxf(pm, dpp<XOR8>(pm));
  const float mn = fmaxf(m, pm);
  const float alpha = expd(m, mn), pr = expd(sc, mn);
  l = fmaf(l, alpha, pr); m = mn;
  const float a0 = dpp<0x00>(alpha), a1 = dpp<0x55>(alpha), a2 = dpp<0xAA>(alpha), a3 = dpp<0xFF>(alpha);
#pragma unroll
  for (int k = 0; k < 8; k += 2) {
    const float al[4] = {a0, a1, a2, a3};
#pragma unroll
    for (int g = 0; g < 4; ++g) {
      f32x2 t = f32x2{acc[8 * g + k], acc[8 * g + k + 1]} * f32x2{al[g], al[g]};
      acc[8 * g + k] = t[0]; acc[8 * g + k + 1] = t[1];
    }
  }
  return pr;
}
template <int T> __device__ __forceinline__ void pv_step(const u32x4& v, float pr, float (&acc)[32]) {
  float vf[8];
#pragma unroll
  for (int k = 0; k < 4; ++k) { vf[2 * k] = bflo(v[k]); vf[2 * k + 1] = bfhi(v[k]); }
  const float p0 = dpp<NEWBC + 4 * T + 0>(pr), p1 = dpp<NEWBC + 4 * T + 1>(pr), p2 = dpp<NEWBC + 4 * T + 2>(pr), p3 = dpp<NEWBC + 4 * T + 3>(pr);
  const float pp[4] = {p0, p1, p2, p3};
#pragma unroll
  for (int k = 0; k < 8; k += 2)
#pragma unroll
    for (int g = 0; g < 4; ++g) {
      const f32x2 t = __builtin_elementwise_fma(f32x2{pp[g], pp[g]}, f32x2{vf[k], vf[k + 1]}, f32x2{acc[8 * g + k], acc[8 * g + k + 1]});
      acc[8 * g + k] = t[0]; acc[8 * g + k + 1] = t[1];
    }
}
__device__ __forceinline__ void qk4(const u32x4& k, const u32x4 (&q)[4], float* x) {
#pragma unroll
  for (int g = 0; g < 4; ++g) {
    float d = 0.f;
#pragma unroll
    for (int i = 0; i < 4; ++i) d = dot2(k[i], q[g][i], d);
    x[g] = d;
  }
}

template <int NP> __device__ __forceinline__ f32x4 proj_tile(const u32x4 (&wt)[2 * NP], const unsigned char* act, int row0, bool b3s) {
  float y[32];
#pragma unroll
  for (int i = 0; i < 32; ++i) y[i] = 0.f;
#pragma unroll
  for (int pp = 0; pp < NP; ++pp) {
    unsigned apk[4];
#pragma unroll
    for (int b = 0; b < 4; ++b) apk[b] = *(const unsigned*)(act + (b * DM + row0 + 16 * pp) * 2);
#pragma unroll
    for (int k = 0; k < 4; ++k) {
      const unsigned lo = __builtin_amdgcn_perm(wt[2 * pp + 1][k], wt[2 * pp][k], 0x05040100u);
      const unsigned hi = __builtin_amdgcn_perm(wt[2 * pp + 1][k], wt[2 * pp][k], 0x07060302u);
#pragma unroll
      for (int b = 0; b < 4; ++b) { y[b * 8 + 2 * k] = dot2(lo, apk[b], y[b * 8 + 2 * k]); y[b * 8 + 2 * k + 1] = dot2(hi, apk[b], y[b * 8 + 2 * k + 1]); }
    }
  }
#pragma unroll
  for (int h = 0; h < 16; ++h) y[h] = swadd32(y[h], y[h + 16]);
#pragma unroll
  for (int h = 0; h < 8; ++h) y[h] = swadd16(y[h], y[h + 8]);
#pragma unroll
  for (int h = 0; h < 4; ++h) y[h] = trsel<XOR8>(y[h], y[h + 4], b3s);
  return f32x4{y[0], y[1], y[2], y[3]};
}
__device__ __forceinline__ void lds_arrive(LAS unsigned* cnt, int lane) {
  asm volatile("s_waitcnt lgkmcnt(0)" ::: "memory");
  if (lane == 0) __hip_atomic_fetch_add(cnt, 1u, __ATOMIC_RELAXED, __HIP_MEMORY_SCOPE_WORKGROUP);
}
template <int SLEEP> __device__ __forceinline__ void lds_wait(LAS unsigned* cnt, unsigned target) {
#pragma nounroll
  for (unsigned spins = 0; spins < (1u << 21) / (SLEEP + 1); ++spins) {
    const unsigned v = __builtin_amdgcn_readfirstlane(__hip_atomic_load(cnt, __ATOMIC_RELAXED, __HIP_MEMORY_SCOPE_WORKGROUP));
    if (v >= target) break;
    if (SLEEP) __builtin_amdgcn_s_sleep(SLEEP);
  }
  asm volatile("" ::: "memory");
}

__global__ void __launch_bounds__(NT, 4) fused_decode_kernel(Params p) {
  __shared__ __attribute__((aligned(16))) unsigned char lds[L_END];
  unsigned tstart = (unsigned)__builtin_amdgcn_s_memrealtime();
  const int bid = blockIdx.x, tid = threadIdx.x, lane = tid & 63;
  const int w = __builtin_amdgcn_readfirstlane(tid >> 6);
  const int r = lane >> 4, c = lane & 15;
  const int xg = bid & 7, yg = bid >> 3;
  LAS unsigned* sync_cnt = (LAS unsigned*)(lds + L_TAG + 8);
  LAS unsigned* q_cnt = (LAS unsigned*)(lds + L_TAG + 12);
  LAS unsigned* tk_take = (LAS unsigned*)(lds + L_TAG + 16);
  LAS unsigned* tk_give = (LAS unsigned*)(lds + L_TAG + 20);
  LAS unsigned* op_cnt = (LAS unsigned*)(lds + L_TAG + 24);
  LAS unsigned* rem_cnt = (LAS unsigned*)(lds + L_TAG + 28);
  LAS unsigned* slow = (LAS unsigned*)(lds + L_TAG + 32);
#define TICKET() do { unsigned tk_ = 0; \
    if (lane == 0) tk_ = __hip_atomic_fetch_add(tk_take, 1u, __ATOMIC_RELAXED, __HIP_MEMORY_SCOPE_WORKGROUP); \
    tk_ = __builtin_amdgcn_readfirstlane(tk_); \
    lds_wait<1>(tk_give, (unsigned)max(0, (int)tk_ + 1 - WINDOW)); __builtin_amdgcn_sched_barrier(0); } while (0)
#define LANDED() do { asm volatile("" :: "v"(kr[0]), "v"(kr[1]), "v"(kr[2]), "v"(kr[3])); \
    if (lane == 0) __hip_atomic_fetch_add(tk_give, 1u, __ATOMIC_RELAXED, __HIP_MEMORY_SCOPE_WORKGROUP); __builtin_amdgcn_sched_barrier(0); } while (0)
  const __amdgpu_buffer_rsrc_t rws = __builtin_amdgcn_make_buffer_rsrc((void*)p.ws, 0, WS_END, RSRC_W3);
  const __amdgpu_buffer_rsrc_t rx = __builtin_amdgcn_make_buffer_rsrc((void*)p.x, 0, NB * DM * 2, RSRC_W3);
  const int ab = yg >> 3, akv = xg, asp = yg & 7;
  const size_t kvbase = (size_t)(ab * HKV + akv) * SK * DH;
  const __amdgpu_buffer_rsrc_t rk = __builtin_amdgcn_make_buffer_rsrc((void*)(p.kc + kvbase), 0, PAGES * 4096, RSRC_W3);
  const __amdgpu_buffer_rsrc_t rv = __builtin_amdgcn_make_buffer_rsrc((void*)(p.vc + kvbase), 0, PAGES * 4096, RSRC_W3);
  const int voff = lane * 16;
#define PAGE_OF(i_) ((((((int)(i_)) >> 4) + ab * 2 + akv * 3) & 7) * 128 + asp * 16 + (((int)(i_)) & 15))
  const int i0 = w < 12 ? w - 4 : 120 + 2 * (w - 12), istep = w < 12 ? 8 : 1;
  const unsigned iend = w < 12 ? 120u : (unsigned)(i0 + 2);
  const bool b0s = lane & 1, b1s = lane & 2, b2s = lane & 4, b3s = lane & 8;
  const int oh = yg, onc = xg;
  const int okc = akv * 8 + (yg >> 2), onq = yg & 3;
#define LOAD_PAGE(so_) do { \
    _Pragma("unroll") for (int t = 0; t < 4; ++t) kr[t] = __builtin_amdgcn_raw_buffer_load_b128(rk, voff + t * 1024, (so_), AUX_NT); \
    _Pragma("unroll") for (int t = 0; t < 4; ++t) vr[t] = __builtin_amdgcn_raw_buffer_load_b128(rv, voff + t * 1024, (so_), AUX_NT); } while (0)
#define MAKE_TAG(e0_, e1_) do { \
    epoch = ((u64)(unsigned)__builtin_amdgcn_readfirstlane(e1_) << 32) | (u64)(unsigned)__builtin_amdgcn_readfirstlane(e0_); \
    const u64 tg_ = (epoch * 0x9E3779B97F4A7C15ull + 0xD1B54A32D192ED03ull) >> 24; tlo = (unsigned)(tg_ & 0xffu) << 24; thi = (unsigned)(tg_ >> 8); } while (0)
#define BAD2(v_) (gbad((v_)[0], (v_)[1], tlo, thi) | gbad((v_)[2], (v_)[3], tlo, thi))

  u32x4 kr[4], vr[4];
  unsigned tlo, thi;
  u64 epoch;
  if (w < 4) {
    __builtin_amdgcn_s_setprio(3);
    const int cn = lane & 7, rp = lane >> 3;
    const int qrow = (yg >> 3) * 128 + w * 32 + 2 * rp, qcol = xg * 512 + (yg & 7) * 64;
    const int hrow = xg * 64 + w * 16 + 2 * rp, hcol = yg * 64;
    const bool has_mlp = yg < 28;
    if (tid == 0) { *sync_cnt = 0u; *q_cnt = 0u; *tk_take = W0; *tk_give = 0u; *op_cnt = 0u; *rem_cnt = 0u; *slow = 0u; }
    const u32x4 xa = __builtin_amdgcn_raw_buffer_load_b128(rx, (w * DM + lane * 8) * 2, 0, 0);
    u32x4 wqv[4], w1v[2];
    {
      const __amdgpu_buffer_rsrc_t rq = __builtin_amdgcn_make_buffer_rsrc((void*)p.wq, 0, DM * 4096 * 2, RSRC_W3);
      const int o = (qrow * 4096 + qcol + cn * 8) * 2;
      wqv[0] = __builtin_amdgcn_raw_buffer_load_b128(rq, o, 0, AUX_NT);
      wqv[1] = __builtin_amdgcn_raw_buffer_load_b128(rq, o + 8192, 0, AUX_NT);
      wqv[2] = __builtin_amdgcn_raw_buffer_load_b128(rq, o + 16 * 8192, 0, AUX_NT);
      wqv[3] = __builtin_amdgcn_raw_buffer_load_b128(rq, o + 17 * 8192, 0, AUX_NT);
    }
    const u32x2 e = __builtin_amdgcn_raw_buffer_load_b64(rws, w == 0 ? WS_EPOCH : OOB, 0, AUX_SC1);
    {
      const __amdgpu_buffer_rsrc_t r1s = __builtin_amdgcn_make_buffer_rsrc((void*)p.w1, 0, DM * FF * 2, RSRC_W3);
      const int o = has_mlp ? (hrow * FF + hcol + cn * 8) * 2 : OOB;
      w1v[0] = __builtin_amdgcn_raw_buffer_load_b128(r1s, o, 0, AUX_NT);
      w1v[1] = __builtin_amdgcn_raw_buffer_load_b128(r1s, o + FF * 2, 0, AUX_NT);
    }
    __builtin_amdgcn_sched_barrier(0);
    LDS_BARRIER();
    __builtin_amdgcn_sched_barrier(0);
    float lnv[8];
#pragma unroll
    for (int i = 0; i < 4; ++i) { lnv[2 * i] = bflo(xa[i]); lnv[2 * i + 1] = bfhi(xa[i]); }
    layernorm8(lnv);
    *(u32x4*)(lds + L_XN + (w * DM + lane * 8) * 2) = pack8(lnv);
    lds_arrive(sync_cnt, lane);
    lds_wait<0>(sync_cnt, 4);
    {
      const f32x4 y = proj_tile<2>(wqv, lds + L_XN, qrow, b3s);
      *(f32x4*)(lds + L_RQ + ((w * 4 + r) * 64 + cn * 8 + (b3s ? 4 : 0)) * 4) = y;
    }
    if (tid == 0) *(u32x2*)(lds + L_TAG) = e;
    lds_arrive(sync_cnt, lane);
    lds_wait<0>(sync_cnt, 8);
    {
      const u32x2 ep = *(const u32x2*)(lds + L_TAG);
      MAKE_TAG(ep[0], ep[1]);
      const float* rq = (const float*)(lds + L_RQ) + w * 64 + lane;
      const float sq = (rq[0] + rq[256]) + (rq[512] + rq[768]);
      __builtin_amdgcn_raw_buffer_store_b64(gpack(sq, tlo, thi), rws, WS_QG + (((yg >> 3) * NB + w) * 4096 + qcol + lane) * 8, 0, AUX_SC1);
      __builtin_amdgcn_raw_buffer_store_b64(u32x2{(tstart & 0xffffffu) | tlo, thi}, rws,
                                            (w == 0 && bid < 64 && lane < 8) ? WS_ORG + lane * 512 + bid * 8 : OOB, 0, AUX_SC1);
    }
    layernorm8(lnv);
    *(u32x4*)(lds + L_Y + (w * DM + lane * 8) * 2) = pack8(lnv);
    lds_arrive(sync_cnt, lane);
    lds_wait<0>(sync_cnt, 12);
    {
      const f32x4 y = proj_tile<1>(w1v, lds + L_Y, hrow, b3s);
      *(f32x4*)(lds + L_RH + ((w * 4 + r) * 64 + cn * 8 + (b3s ? 4 : 0)) * 4) = y;
    }
    lds_arrive(sync_cnt, lane);
    u32x4 qg[4];
    const int qgo = WS_QG + (ab * 4096 + akv * 512 + w * 128) * 8 + lane * 16;
    constexpr int QSTR = NB * 4096 * 8;
#pragma unroll
    for (int i = 0; i < 4; ++i) qg[i] = __builtin_amdgcn_raw_buffer_load_b128(rws, qgo + i * QSTR, 0, AUX_SC1);
    const u32x2 og = __builtin_amdgcn_raw_buffer_load_b64(rws, w == 1 ? WS_ORG + xg * 512 + lane * 8 : OOB, 0, AUX_SC1);
    __builtin_amdgcn_sched_barrier(0);
    lds_wait<0>(sync_cnt, 16);
    {
      const float* rh = (const float*)(lds + L_RH) + w * 64 + lane;
      const float sh = (rh[0] + rh[256]) + (rh[512] + rh[768]);
      __builtin_amdgcn_raw_buffer_store_b64(gpack(sh, tlo, thi), rws, has_mlp ? WS_HG + ((xg * NB + w) * FF + hcol + lane) * 8 : OOB, 0, AUX_SC1);
    }
    for (unsigned spins = 0;; ++spins) {
      unsigned bad = 0;
#pragma unroll
      for (int i = 0; i < 4; ++i) bad |= gbad(qg[i][0], qg[i][1], tlo, thi) | gbad(qg[i][2], qg[i][3], tlo, thi);
      if (__builtin_amdgcn_ballot_w64(bad != 0) == 0ull || spins > SPIN_CAP) break;
      asm volatile("" ::: "memory");
#pragma unroll
      for (int i = 0; i < 4; ++i) qg[i] = __builtin_amdgcn_raw_buffer_load_b128(rws, qgo + i * QSTR, 0, AUX_SC1);
    }
    {
      const float q0 = (gval(qg[0][0]) + gval(qg[1][0])) + (gval(qg[2][0]) + gval(qg[3][0]));
      const float q1 = (gval(qg[0][2]) + gval(qg[1][2])) + (gval(qg[2][2]) + gval(qg[3][2]));
      ((unsigned*)(lds + L_Q))[w * 64 + lane] = bfbits(q0) | (bfbits(q1) << 16);
    }
    if (w == 1) {
      const unsigned dt = (tstart - og[0]) & 0xffffffu;
      float a = (gbad(og[0], og[1], tlo, thi) == 0u && dt < (unsigned)ORGMAX) ? (float)dt : 0.f;
      a = fmaxf(a, dpp<XOR1>(a)); a = fmaxf(a, dpp<XOR2>(a)); a = fmaxf(a, dpp<XOR7>(a)); a = fmaxf(a, dpp<XOR8>(a));
      a = swmax32(swmax16(a));
      if (lane == 0) *(unsigned*)(lds + L_TAG + 36) = tstart - (unsigned)a;
    }
    lds_arrive(q_cnt, lane);
    __builtin_amdgcn_s_setprio(0);
#pragma unroll
    for (int t = 0; t < 4; ++t) { kr[t] = u32x4{0u, 0u, 0u, 0u}; vr[t] = u32x4{0u, 0u, 0u, 0u}; }
  } else {
    if (w >= 4 + W0) {
      __builtin_amdgcn_s_barrier();
      asm volatile("" ::: "memory");
      TICKET();
    }
    LOAD_PAGE(PAGE_OF(i0) * 4096);
    __builtin_amdgcn_sched_barrier(0);
    if (w < 4 + W0) {
      __builtin_amdgcn_s_barrier();
      asm volatile("" ::: "memory");
    }
    const bool is_wo = w < 12;
    const int kp = is_wo ? (tid - 256) >> 4 : (tid - 768) >> 3, cg = is_wo ? tid & 15 : tid & 7;
    const __amdgpu_buffer_rsrc_t rw = __builtin_amdgcn_make_buffer_rsrc((void*)(is_wo ? p.wo : p.w2), 0, is_wo ? 4096 * DM * 2 : FF * DM * 2, RSRC_W3);
    const int oo = is_wo ? ((okc * 64 + 2 * kp) * DM + onq * 128 + cg * 8) * 2
                         : (kp < 28 ? ((oh * 56 + 2 * kp) * DM + onc * 64 + cg * 8) * 2 : OOB);
    const u32x4 ua = __builtin_amdgcn_raw_buffer_load_b128(rw, oo, 0, AUX_NT);
    const u32x4 ub = __builtin_amdgcn_raw_buffer_load_b128(rw, oo + DM * 2, 0, AUX_NT);
    __builtin_amdgcn_sched_barrier(0);
    LANDED();
    unsigned char* pk = lds + L_PARK + (w - 4) * 8192 + voff;
#pragma unroll
    for (int t = 0; t < 4; ++t) *(u32x4*)(pk + t * 1024) = kr[t];
#pragma unroll
    for (int t = 0; t < 4; ++t) *(u32x4*)(pk + 4096 + t * 1024) = vr[t];
    __builtin_amdgcn_sched_barrier(0);
#pragma unroll
    for (int j = 0; j < 8; ++j) {
      const int ln = 4 * cg + (j >> 1), ch = (j & 1) * 8 + (kp >> 2);
      const int dst = is_wo ? L_WOT + ln * 256 + ((ch ^ (ln & 15)) << 4) + (kp & 3) * 4
                            : L_W2T + (j * 8 + cg) * 144 + kp * 4;
      *(unsigned*)(lds + dst) = __builtin_amdgcn_perm(ub[j >> 1], ua[j >> 1], (j & 1) ? 0x07060302u : 0x05040100u);
    }
    TICKET();
    LOAD_PAGE(PAGE_OF(i0 + istep) * 4096);
    __builtin_amdgcn_sched_barrier(0);
    LANDED();
  }
  lds_wait<2>(q_cnt, 4);
  {
    const u32x2 ep = *(const u32x2*)(lds + L_TAG);
    MAKE_TAG(ep[0], ep[1]);
    tstart = __builtin_amdgcn_readfirstlane(*(const unsigned*)(lds + L_TAG + 36));
  }

  if (w >= 4) {
    float m = -INFINITY, l = 0.f, acc[32];
#pragma unroll
    for (int k = 0; k < 32; ++k) acc[k] = 0.f;
    u32x4 q[4];
#pragma unroll
    for (int g = 0; g < 4; ++g) q[g] = *(const u32x4*)(lds + L_Q + g * 256 + c * 16);
    unsigned nxt = (unsigned)(i0 + 2 * istep);
    unsigned parkat = nxt;
    asm volatile("" : "+v"(parkat));
    parkat = __builtin_amdgcn_readfirstlane(parkat);
    bool more;
    do {
      float x[16];
#pragma unroll
      for (int t = 0; t < 4; ++t) qk4(kr[t], q, x + 4 * t);
      PIN8(x, 0); PIN8(x, 8);
      const unsigned slv = __hip_atomic_load(slow, __ATOMIC_RELAXED, __HIP_MEMORY_SCOPE_WORKGROUP);
      more = nxt < iend;
      const int so = more ? PAGE_OF(nxt) * 4096 : OOB;
      if (more) {
        const int due = (int)((__builtin_amdgcn_readfirstlane(slv) ? (unsigned)(TB0 + nxt * TAUB) : (unsigned)(TA0 + nxt * TAU)) >> 4);
#pragma nounroll
        for (unsigned sp = 0; sp < 4096u; ++sp) {
          if ((int)((unsigned)__builtin_amdgcn_s_memrealtime() - tstart) >= due) break;
          __builtin_amdgcn_s_sleep(1);
        }
      }
      __builtin_amdgcn_sched_barrier(0);
#pragma unroll
      for (int t = 0; t < 4; ++t) kr[t] = __builtin_amdgcn_raw_buffer_load_b128(rk, voff + t * 1024, so, AUX_NT);
      __builtin_amdgcn_sched_barrier(0);
      const float pr = softmax_step(x, m, l, acc, b0s, b1s, b2s, b3s);
      pv_step<0>(vr[0], pr, acc); pv_step<1>(vr[1], pr, acc); pv_step<2>(vr[2], pr, acc); pv_step<3>(vr[3], pr, acc);
      PIN8(acc, 0); PIN8(acc, 8); PIN8(acc, 16); PIN8(acc, 24);
      __builtin_amdgcn_sched_barrier(0);
#pragma unroll
      for (int t = 0; t < 4; ++t) vr[t] = __builtin_amdgcn_raw_buffer_load_b128(rv, voff + t * 1024, so, AUX_NT);
      __builtin_amdgcn_sched_barrier(0);
      if (istep == 8) {
        unsigned old = 0;
        if (lane == 0) old = __hip_atomic_fetch_add(rem_cnt, 1u, __ATOMIC_RELAXED, __HIP_MEMORY_SCOPE_WORKGROUP);
        old = __builtin_amdgcn_readfirstlane(old);
        if (nxt - 16u > old) __builtin_amdgcn_s_setprio(0); else __builtin_amdgcn_s_setprio(1);
      }
      if (nxt == parkat) {
        const unsigned char* pk = lds + L_PARK + (w - 4) * 8192 + voff;
        float y[16];
#pragma unroll
        for (int t = 0; t < 4; ++t) { const u32x4 kk = *(const u32x4*)(pk + t * 1024); qk4(kk, q, y + 4 * t); }
        const float p2 = softmax_step(y, m, l, acc, b0s, b1s, b2s, b3s);
        { const u32x4 vv = *(const u32x4*)(pk + 4096); pv_step<0>(vv, p2, acc); }
        { const u32x4 vv = *(const u32x4*)(pk + 4096 + 1024); pv_step<1>(vv, p2, acc); }
        { const u32x4 vv = *(const u32x4*)(pk + 4096 + 2048); pv_step<2>(vv, p2, acc); }
        { const u32x4 vv = *(const u32x4*)(pk + 4096 + 3072); pv_step<3>(vv, p2, acc); }
        PIN8(acc, 0); PIN8(acc, 8); PIN8(acc, 16); PIN8(acc, 24);
        __builtin_amdgcn_sched_barrier(0);
      }
      nxt += istep;
    } while (more);
    l += dpp<ROR4>(l); l += dpp<XOR8>(l);
    const float M = swmax32(swmax16(m));
    const float f = expd(m, M);
    l *= f;
    l = swadd16(l, l); l = swadd32(l, l);
    const float a0 = dpp<0x00>(f), a1 = dpp<0x55>(f), a2 = dpp<0xAA>(f), a3 = dpp<0xFF>(f);
#pragma unroll
    for (int k = 0; k < 8; ++k) { acc[k] *= a0; acc[8 + k] *= a1; acc[16 + k] *= a2; acc[24 + k] *= a3; }
#pragma unroll
    for (int k = 0; k < 16; ++k) acc[k] = swadd32(acc[k], acc[k + 16]);
#pragma unroll
    for (int k = 0; k < 8; ++k) acc[k] = swadd16(acc[k], acc[k + 8]);
    float* rec = (float*)(lds + (w < 12 ? L_CM + (w - 4) * 2048 : L_PARK + (w - 4) * 8192)) + r * 128 + 8 * c;
    *(f32x4*)rec = f32x4{acc[0], acc[1], acc[2], acc[3]};
    *(f32x4*)(rec + 4) = f32x4{acc[4], acc[5], acc[6], acc[7]};
    if (lane < 4) { ((float*)(lds + L_MM))[lane * 16 + w] = M; ((float*)(lds + L_LL))[lane * 16 + w] = l; }
  } else if (w == 0) {
#pragma nounroll
    for (int ck = 0; ck < NCK; ++ck) {
      const int tc = T1 + ck * TCK;
#pragma nounroll
      for (unsigned sp = 0; sp < 4096u; ++sp) {
        if ((int)((unsigned)__builtin_amdgcn_s_memrealtime() - tstart) >= tc) break;
        __builtin_amdgcn_s_sleep(8);
      }
      const unsigned prog = __hip_atomic_load(rem_cnt, __ATOMIC_RELAXED, __HIP_MEMORY_SCOPE_WORKGROUP);
      const unsigned want = (unsigned)((tc * 16 - TA0) / TAU - 16 - LAGP);
      __builtin_amdgcn_raw_buffer_store_b64(u32x2{1u | tlo, thi}, rws,
          (asp == 0 && lane < 8 && prog < want) ? WS_LAG + ck * 2048 + lane * 256 + (ab * 8 + akv) * 8 : OOB, 0, AUX_SC1);
#pragma nounroll
      for (unsigned sp = 0; sp < 4096u; ++sp) {
        if ((int)((unsigned)__builtin_amdgcn_s_memrealtime() - tstart) >= tc + TVD) break;
        __builtin_amdgcn_s_sleep(8);
      }
      const u32x2 vt = __builtin_amdgcn_raw_buffer_load_b64(rws, lane < 32 ? WS_LAG + ck * 2048 + xg * 256 + lane * 8 : OOB, 0, AUX_SC1);
      const int votes = __builtin_popcountll(__builtin_amdgcn_ballot_w64(lane < 32 && gbad(vt[0], vt[1], tlo, thi) == 0u));
      if (votes >= VOTES) {
        if (lane == 0) __hip_atomic_store(slow, 1u, __ATOMIC_RELAXED, __HIP_MEMORY_SCOPE_WORKGROUP);
        break;
      }
    }
    __builtin_amdgcn_s_waitcnt(0x0f70);
  }
  __builtin_amdgcn_sched_barrier(0);
  LDS_BARRIER();
  __builtin_amdgcn_sched_barrier(0);
  asm volatile("" :: "v"(kr[0]), "v"(kr[1]), "v"(kr[2]), "v"(kr[3]), "v"(vr[0]), "v"(vr[1]), "v"(vr[2]), "v"(vr[3]));
  if (w < 4 || w >= 12) {
    const int mi = w < 4 ? w : w - 8, g = mi >> 1, e = mi * 64 + lane, d = e & 127;
    f32x4 mv[3], lv[3];
    float rv12[12];
#pragma unroll
    for (int i = 0; i < 3; ++i) mv[i] = *(const f32x4*)(lds + L_MM + g * 64 + 16 + i * 16);
#pragma unroll
    for (int i = 0; i < 12; ++i) rv12[i] = *(const float*)(lds + (i < 8 ? L_CM + i * 2048 : L_PARK + i * 8192) + e * 4);
#pragma unroll
    for (int i = 0; i < 3; ++i) lv[i] = *(const f32x4*)(lds + L_LL + g * 64 + 16 + i * 16);
    __builtin_amdgcn_sched_barrier(0);
    float M = -INFINITY;
#pragma unroll
    for (int i = 0; i < 12; ++i) M = fmaxf(M, mv[i >> 2][i & 3]);
    float L0 = 0.f, L1 = 0.f, o0 = 0.f, o1 = 0.f;
#pragma unroll
    for (int i = 0; i < 12; i += 2) {
      const float w0 = expd(mv[i >> 2][i & 3], M), w1 = expd(mv[i >> 2][(i & 3) + 1], M);
      o0 = fmaf(w0, rv12[i], o0); o1 = fmaf(w1, rv12[i + 1], o1);
      L0 = fmaf(w0, lv[i >> 2][i & 3], L0); L1 = fmaf(w1, lv[i >> 2][(i & 3) + 1], L1);
    }
    const int rec = (ab * HQ + akv * 4 + g) * 8 + asp;
    __builtin_amdgcn_raw_buffer_store_b64(gpack(o0 + o1, tlo, thi), rws, WS_PG + (rec * 128 + d) * 8, 0, AUX_SC1);
    __builtin_amdgcn_raw_buffer_store_b64(gpack(b0s ? L0 + L1 : M, tlo, thi), rws, lane < 16 ? WS_ML + (rec * 32 + (mi & 1) * 16 + lane) * 8 : OOB, 0, AUX_SC1);
    if (w == 12) {
      const int fb = bid >> 6, n0 = (bid & 63) * 8 + 2 * (lane & 3), kq = lane >> 2;
      const int so = WS_SG + ((kq * NB + fb) * DM + n0) * 8, mo = WS_MG + ((kq * NB + fb) * DM + n0) * 8;
      constexpr int KSTR = 16 * NB * DM * 8;
      const __amdgpu_buffer_rsrc_t rb = __builtin_amdgcn_make_buffer_rsrc((void*)p.b2, 0, DM * 2, RSRC_W3);
      const unsigned b2v = __builtin_amdgcn_raw_buffer_load_b32(rb, n0 * 2, 0, 0);
      const unsigned xv = __builtin_amdgcn_raw_buffer_load_b32(rx, (fb * DM + n0) * 2, 0, 0);
      lds_wait<2>(op_cnt, 4);
      u32x4 sa[6], sb[6];
      float fv[4];
#define F_LOAD(P) do { _Pragma("unroll") for (int i = 0; i < 4; ++i) P[i] = __builtin_amdgcn_raw_buffer_load_b128(rws, so, i * KSTR, AUX_SC1); \
        _Pragma("unroll") for (int i = 0; i < 2; ++i) P[4 + i] = __builtin_amdgcn_raw_buffer_load_b128(rws, mo, i * KSTR, AUX_SC1); \
        __builtin_amdgcn_sched_barrier(0); asm volatile("" ::: "memory"); } while (0)
#define F_TAKE(P) do { fv[0] = (gval(P[0][0]) + gval(P[1][0])) + (gval(P[2][0]) + gval(P[3][0])); \
        fv[1] = (gval(P[0][2]) + gval(P[1][2])) + (gval(P[2][2]) + gval(P[3][2])); \
        fv[2] = gval(P[4][0]) + gval(P[5][0]); fv[3] = gval(P[4][2]) + gval(P[5][2]); \
        asm volatile("" : "+v"(fv[0]), "+v"(fv[1]), "+v"(fv[2]), "+v"(fv[3])); } while (0)
#define F_OK(P) (__builtin_amdgcn_ballot_w64((BAD2(P[0]) | BAD2(P[1]) | BAD2(P[2]) | BAD2(P[3]) | BAD2(P[4]) | BAD2(P[5])) != 0) == 0ull)
      __builtin_amdgcn_s_sleep(FDEL);
      F_LOAD(sb);
      __builtin_amdgcn_s_sleep(GSTAG);
#pragma nounroll
      for (unsigned spins = 0;; ++spins) {
        F_LOAD(sa);
        F_TAKE(sb);
        if (F_OK(sb)) break;
        F_LOAD(sb);
        F_TAKE(sa);
        if (F_OK(sa) || spins >= SPIN_CAP) break;
      }
#pragma unroll
      for (int i = 0; i < 4; ++i) {
        float t = fv[i];
        t += dpp<ROR4>(t); t += dpp<XOR8>(t);
        t = swadd16(t, t); t = swadd32(t, t);
        fv[i] = t;
      }
      float o[2];
#pragma unroll
      for (int j = 0; j < 2; ++j) {
        const float mm = bfr(bfr(fv[2 + j]) + (j ? bfhi(b2v) : bflo(b2v)));
        const float gt = bfr(mm * __builtin_amdgcn_rcpf(1.0f + __expf(-mm)));
        o[j] = (j ? bfhi(xv) : bflo(xv)) + bfr(bfr(fv[j]) * gt);
      }
      if (lane < 4) *(unsigned*)(p.out + fb * DM + n0) = bfbits(o[0]) | (bfbits(o[1]) << 16);
      if (bid == 0 && lane == 0) __hip_atomic_store((GAS u64*)(p.ws + WS_EPOCH), epoch + 1ull, RLX_AGENT);
      __builtin_amdgcn_sched_barrier(0);
      asm volatile("" :: "v"(sa[0]), "v"(sa[1]), "v"(sa[2]), "v"(sa[3]), "v"(sa[4]), "v"(sa[5]),
                         "v"(sb[0]), "v"(sb[1]), "v"(sb[2]), "v"(sb[3]), "v"(sb[4]), "v"(sb[5]));
    }
  } else if (w < 8) {
    const int gb = w - 4, sp = lane >> 3, dd = lane & 7;
    const int crec = (gb * HQ + (okc >> 1)) * 8 + sp;
    const int po = WS_PG + (crec * 128 + (okc & 1) * 64) * 8 + dd * 16, mo = WS_ML + (crec * 2 + (okc & 1)) * 128;
    u32x4 ga[5], gb5[5];
    float gv[10];
#define G_LOAD(P) do { P[4] = __builtin_amdgcn_raw_buffer_load_b128(rws, mo, 0, AUX_SC1); \
      _Pragma("unroll") for (int i = 0; i < 4; ++i) P[i] = __builtin_amdgcn_raw_buffer_load_b128(rws, po + i * 128, 0, AUX_SC1); \
      __builtin_amdgcn_sched_barrier(0); asm volatile("" ::: "memory"); } while (0)
#define G_TAKE(P) do { _Pragma("unroll") for (int i = 0; i < 5; ++i) { gv[2 * i] = gval(P[i][0]); gv[2 * i + 1] = gval(P[i][2]); } \
      asm volatile("" : "+v"(gv[0]), "+v"(gv[1]), "+v"(gv[2]), "+v"(gv[3]), "+v"(gv[4]), "+v"(gv[5]), "+v"(gv[6]), "+v"(gv[7]), "+v"(gv[8]), "+v"(gv[9])); } while (0)
#define G_OK(P) (__builtin_amdgcn_ballot_w64((BAD2(P[0]) | BAD2(P[1]) | BAD2(P[2]) | BAD2(P[3]) | BAD2(P[4])) != 0) == 0ull)
    u32x4 wc[8];
#pragma unroll
    for (int i = 0; i < 8; ++i) wc[i] = *(const u32x4*)(lds + L_WOT + lane * 256 + ((i ^ c) << 4));
    asm volatile("" :: "v"(wc[0]), "v"(wc[1]), "v"(wc[2]), "v"(wc[3]), "v"(wc[4]), "v"(wc[5]), "v"(wc[6]), "v"(wc[7]));
    __builtin_amdgcn_s_sleep(GDEL);
    G_LOAD(gb5);
    __builtin_amdgcn_s_sleep(GSTAG);
#pragma nounroll
    for (unsigned spins = 0;; ++spins) {
      G_LOAD(ga);
      G_TAKE(gb5);
      if (G_OK(gb5)) break;
      G_LOAD(gb5);
      G_TAKE(ga);
      if (G_OK(ga) || spins >= SPIN_CAP) break;
    }
    const float ms = gv[8], ls = gv[9];
    float Mx = fmaxf(ms, dpp<XOR8>(ms));
    Mx = swmax32(swmax16(Mx));
    const float wt = expd(ms, Mx);
    float Ls = wt * ls;
    Ls += dpp<XOR8>(Ls); Ls = swadd16(Ls, Ls); Ls = swadd32(Ls, Ls);
    float v[8];
#pragma unroll
    for (int i = 0; i < 8; ++i) v[i] = wt * gv[i];
#pragma unroll
    for (int i = 0; i < 4; ++i) v[i] = swadd32(v[i], v[i + 4]);
#pragma unroll
    for (int i = 0; i < 2; ++i) v[i] = swadd16(v[i], v[i + 2]);
    v[0] = trsel<XOR8>(v[0], v[1], b3s);
    ((u16*)(lds + L_O + gb * 128))[2 * dd + (b3s ? 1 : 0) + 16 * r] = (u16)bfbits(v[0] * __builtin_amdgcn_rcpf(Ls));
    asm volatile("" ::: "memory");
    float s0 = 0.f, s1 = 0.f, s2 = 0.f, s3 = 0.f;
#pragma unroll
    for (int i = 0; i < 8; ++i) {
      const u32x4 ov = *(const u32x4*)(lds + L_O + gb * 128 + i * 16);
      const u32x4 wd = *(const u32x4*)(lds + L_WOT + lane * 256 + (((8 + i) ^ c) << 4));
      s0 = dot2(wc[i][0], ov[0], s0); s1 = dot2(wd[0], ov[0], s1); s2 = dot2(wc[i][1], ov[1], s2); s3 = dot2(wd[1], ov[1], s3);
      s0 = dot2(wc[i][2], ov[2], s0); s1 = dot2(wd[2], ov[2], s1); s2 = dot2(wc[i][3], ov[3], s2); s3 = dot2(wd[3], ov[3], s3);
    }
    const u32x2 g0 = gpack(s0 + s2, tlo, thi), g1 = gpack(s1 + s3, tlo, thi);
    __builtin_amdgcn_raw_buffer_store_b128(u32x4{g0[0], g0[1], g1[0], g1[1]}, rws,
                                           WS_SG + ((okc * NB + gb) * DM + onq * 128 + 2 * lane) * 8, 0, AUX_SC1);
    if (lane == 0) __hip_atomic_fetch_add(op_cnt, 1u, __ATOMIC_RELAXED, __HIP_MEMORY_SCOPE_WORKGROUP);
    __builtin_amdgcn_sched_barrier(0);
    asm volatile("" :: "v"(ga[0]), "v"(ga[1]), "v"(ga[2]), "v"(ga[3]), "v"(ga[4]), "v"(gb5[0]), "v"(gb5[1]), "v"(gb5[2]), "v"(gb5[3]), "v"(gb5[4]));
  } else {
    const int tb = w - 8;
    const int hu = oh * 56 + (lane < 56 ? lane : 0);
    const int ho = WS_HG + (tb * FF + hu) * 8;
    constexpr int HSTR = NB * FF * 8;
    const __amdgpu_buffer_rsrc_t rb = __builtin_amdgcn_make_buffer_rsrc((void*)p.b1, 0, FF * 2, RSRC_W3);
    const unsigned bb = __builtin_amdgcn_raw_buffer_load_b16(rb, hu * 2, 0, 0);
    u32x2 hg[8];
#pragma unroll
    for (int i = 0; i < 8; ++i) hg[i] = __builtin_amdgcn_raw_buffer_load_b64(rws, ho + i * HSTR, 0, AUX_SC1);
    __builtin_amdgcn_sched_barrier(0);
    u32x4 wc[7];
#pragma unroll
    for (int i = 0; i < 7; ++i) wc[i] = *(const u32x4*)(lds + L_W2T + lane * 144 + i * 16);
#pragma nounroll
    for (unsigned spins = 0;; ++spins) {
      unsigned bad = 0;
#pragma unroll
      for (int i = 0; i < 8; ++i) bad |= gbad(hg[i][0], hg[i][1], tlo, thi);
      if (__builtin_amdgcn_ballot_w64(bad != 0) == 0ull || spins >= SPIN_CAP) break;
      __builtin_amdgcn_s_sleep(8);
      asm volatile("" ::: "memory");
#pragma unroll
      for (int i = 0; i < 8; ++i) hg[i] = __builtin_amdgcn_raw_buffer_load_b64(rws, ho + i * HSTR, 0, AUX_SC1);
    }
    const float sh = ((gval(hg[0][0]) + gval(hg[1][0])) + (gval(hg[2][0]) + gval(hg[3][0]))) +
                     ((gval(hg[4][0]) + gval(hg[5][0])) + (gval(hg[6][0]) + gval(hg[7][0])));
    const float u = bfr(bfr(sh) + bflo(bb));
    const float z = 0.7978845608028654f * (u + 0.044715f * u * u * u);
    const float th = 1.0f - 2.0f * __builtin_amdgcn_rcpf(__expf(2.0f * z) + 1.0f);
    ((u16*)(lds + L_O + (w - 4) * 128))[lane] = (u16)bfbits(0.5f * u * (1.0f + th));
    asm volatile("" ::: "memory");
    u32x4 ov[7];
#pragma unroll
    for (int i = 0; i < 7; ++i) ov[i] = *(const u32x4*)(lds + L_O + (w - 4) * 128 + i * 16);
    float s0 = 0.f, s1 = 0.f;
#pragma unroll
    for (int i = 0; i < 7; ++i) { s0 = dot2(wc[i][0], ov[i][0], s0); s1 = dot2(wc[i][1], ov[i][1], s1); s0 = dot2(wc[i][2], ov[i][2], s0); s1 = dot2(wc[i][3], ov[i][3], s1); }
    __builtin_amdgcn_raw_buffer_store_b64(gpack(s0 + s1, tlo, thi), rws,
                                          WS_MG + ((oh * NB + tb) * DM + onc * 64 + 8 * (lane & 7) + (lane >> 3)) * 8, 0, AUX_SC1);
  }
}

extern "C" void kernel_launch(void* const* d_in, const int* in_sizes, int n_in,
                              void* d_out, int out_size, void* d_ws, size_t ws_size,
                              hipStream_t stream) {
  Params p;
  p.x = (const u16*)d_in[0]; p.kc = (const u16*)d_in[1]; p.vc = (const u16*)d_in[2];
  p.wq = (const u16*)d_in[3]; p.wo = (const u16*)d_in[4];
  p.w1 = (const u16*)d_in[6]; p.b1 = (const u16*)d_in[7]; p.w2 = (const u16*)d_in[8]; p.b2 = (const u16*)d_in[9];
  p.out = (u16*)d_out; p.ws = (unsigned char*)d_ws;
  hipLaunchKernelGGL(fused_decode_kernel, dim3(NWG), dim3(NT), 0, stream, p);
}
```
